```python
import torch

def _softmax(x, axis=-1):
    return torch.softmax(x, dim=axis)

def _gelu(x):
    return torch.nn.functional.gelu(x, approximate='tanh')

def _silu(x):
    return torch.nn.functional.silu(x)

def _sigmoid(x):
    return torch.sigmoid(x)

def _layernorm(x, axis=-1, eps=1e-05):
    m = x.mean(dim=axis, keepdim=True)
    v = x.var(dim=axis, keepdim=True, unbiased=False)
    return (x - m) / torch.sqrt(v + eps)

def _scatter_add(tgt, idx, src, axis=0):
    return tgt.clone().index_add_(axis, idx, src)

def _segment_sum(src, seg, n):
    out = torch.zeros((n,) + src.shape[1:], dtype=src.dtype)
    return out.index_add_(0, seg, src)

def _pad(x, pw):
    flat = tuple((v for pair in reversed(pw) for v in pair))
    return torch.nn.functional.pad(x, flat)
kernel_name = 'concat_heads__memory_aug_recurrent_mem_tr__mha_block__prompt'

def setup_inputs():

    _w = lambda a: (lambda t: t.to(torch.bfloat16) if t.is_floating_point() else t)(torch.as_tensor(a))

    def _A_setup():
        B, S, Dm = (1, 2048, 512)
        H, Dh = (32, 128)
        M, seg = (8, 32)
        seg = min(seg, S)
        S = S // seg * seg
        return {'x': _w((lambda *s: torch.randn(*s, dtype=torch.bfloat16) * 0.05)(B, S, Dm)), 'mem0': _w((lambda *s: torch.randn(*s, dtype=torch.bfloat16) * 0.8)(B, M, Dm)), 'Wq': _w((lambda *s: torch.randn(*s, dtype=torch.bfloat16) * 1.6)(Dm, H, Dh) * Dm ** (-0.5)), 'Wk': _w((lambda *s: torch.randn(*s, dtype=torch.bfloat16) * 1.6)(Dm, H, Dh) * Dm ** (-0.5)), 'Wv': _w((lambda *s: torch.randn(*s, dtype=torch.bfloat16) * 0.8)(Dm, H, Dh) * Dm ** (-0.5)), 'Wo': _w((lambda *s: torch.randn(*s, dtype=torch.bfloat16) * 0.8)(H, Dh, Dm) * (H * Dh) ** (-0.5)), 'seg_len': seg}
    _A_in = _A_setup()

    def _B_setup():
        B, S, D, H = (1, 2048, 512, 32)
        return {'x': _w((lambda *s: torch.randn(*s, dtype=torch.bfloat16) * 0.2)(B, S, D)), 'wqkv': _w((lambda *s: torch.randn(*s, dtype=torch.bfloat16) * 0.2)(D, 3 * D)), 'wo': _w((lambda *s: torch.randn(*s, dtype=torch.bfloat16) * 0.05)(D, D))}
    _B_in = _B_setup()
    return {'A_x': _A_in['x'], 'A_mem0': _A_in['mem0'], 'A_Wq': _A_in['Wq'], 'A_Wk': _A_in['Wk'], 'A_Wv': _A_in['Wv'], 'A_Wo': _A_in['Wo'], 'A_seg_len': _A_in['seg_len'], 'B_x': _B_in['x'], 'B_wqkv': _B_in['wqkv'], 'B_wo': _B_in['wo']}

def reference(A_x, A_mem0, A_Wq, A_Wk, A_Wv, A_Wo, A_seg_len, B_x, B_wqkv, B_wo):

    def _A_ref(x, mem0, Wq, Wk, Wv, Wo, seg_len):
        B, S, Dm = x.shape
        H, Dh = (Wq.shape[1], Wq.shape[2])
        M = mem0.shape[1]
        scale = Dh ** (-0.5)
        n_seg = S // seg_len
        L = M + seg_len
        idx = torch.arange(L)
        mask = (idx[:, None] < M) | (idx[:, None] >= idx[None, :])
        mem = mem0
        outs = []
        for s in range(n_seg):
            xs = x[:, s * seg_len:(s + 1) * seg_len, :].clone()
            h = torch.concatenate([mem, xs], axis=1)
            q = torch.einsum('btd,dhe->bhte', h, Wq)
            k = torch.einsum('btd,dhe->bhte', h, Wk)
            v = torch.einsum('btd,dhe->bhte', h, Wv)
            scores = torch.einsum('bhqe,bhke->bhqk', q, k) * scale
            scores = torch.where(mask[None, None], scores, -1000000000.0)
            p = _softmax(scores, axis=-1)
            o = torch.einsum('bhqk,bhke->bhqe', p, v)
            o = torch.einsum('bhte,hed->btd', o, Wo)
            mem = o[:, :M, :].clone()
            outs.append(o[:, M:, :])
        return torch.concatenate(outs, axis=1)

    def _B_ref(x, wqkv, wo):
        B, S, D = x.shape
        H = 32
        Dh = D // H
        qkv = (lambda x, *d: x.permute(*d))((x @ wqkv).reshape(B, S, 3, H, Dh), 2, 0, 3, 1, 4)
        q, k, v = (qkv[0], qkv[1], qkv[2])
        scores = torch.einsum('bhqd,bhkd->bhqk', q, k) * Dh ** (-0.5)
        p = _softmax(scores, axis=-1)
        o = torch.einsum('bhqk,bhkd->bhqd', p, v)
        return (lambda x, *d: x.permute(*d))(o, 0, 2, 1, 3).reshape(B, S, D) @ wo
    a = _A_ref(A_x, A_mem0, A_Wq, A_Wk, A_Wv, A_Wo, A_seg_len)
    b = _B_ref(B_x, B_wqkv, B_wo)
    return torch.concatenate([a, b], axis=-1)
```

```cpp
#include <hip/hip_runtime.h>
#include <hip/hip_bf16.h>

#define LAS __attribute__((address_space(3)))
#define DI __device__ __forceinline__
typedef __bf16 bf16x2 __attribute__((ext_vector_type(2)));
typedef __bf16 bf16x8 __attribute__((ext_vector_type(8)));
typedef __bf16 bf16x16 __attribute__((ext_vector_type(16)));
typedef short s16x4 __attribute__((ext_vector_type(4)));
typedef short s16x8 __attribute__((ext_vector_type(8)));
typedef short s16x16 __attribute__((ext_vector_type(16)));
typedef float f32x4 __attribute__((ext_vector_type(4)));
typedef float f32x16 __attribute__((ext_vector_type(16)));
typedef unsigned u32x2 __attribute__((ext_vector_type(2)));
typedef unsigned u32x4 __attribute__((ext_vector_type(4)));
typedef LAS char* lptr;
typedef __amdgpu_buffer_rsrc_t rsrc_t;

#define CLK_K 330
#define CLK_T1 64
#define CLK_O1 -610000
#define CLK_T2 66
#define CLK_O2 -600000
#define CLK_MASK 3
#define RB_UNITS 1

constexpr unsigned MB = 1u << 20;
constexpr unsigned PART_PHYS = 0;
constexpr unsigned MEM_PHYS = MB;
constexpr unsigned F_KVF = MB;
constexpr unsigned F_KMF = MB + 2048;
constexpr unsigned F_PRB = MB + 6656;
constexpr unsigned F_CLM = 2 * MB;
constexpr unsigned F_EPO = 2 * MB + 16384;
constexpr unsigned F_OXF = 2 * MB + 16640;
constexpr unsigned F_OBF = F_OXF + 4096;
constexpr unsigned F_XBF = F_OBF + 256;
constexpr unsigned F_BKF = F_XBF + 256;
constexpr unsigned F_TMO = F_BKF + 256;
constexpr unsigned F_INI_END = F_TMO + 256;
constexpr unsigned F_SEL = F_INI_END;
constexpr unsigned F_RDY = F_SEL + 128;
constexpr unsigned KV_OFF = 3 * MB;
constexpr unsigned KM_OFF = 35 * MB + 32 * 4096;
constexpr unsigned OX_OFF = 44 * MB;
constexpr unsigned OB_OFF = 60 * MB;
constexpr unsigned XB_OFF = 62 * MB;
constexpr unsigned BK_OFF = 64 * MB;
constexpr unsigned WS_BYTES = 68 * MB;
constexpr int LDS_BYTES = 160 * 1024;
constexpr unsigned POISON = 0xFFFFFFFFu;
constexpr int SC1 = 16;

struct Params {
  const __bf16 *ax, *mem0, *wq, *wk, *wv, *wo, *bx, *bwqkv, *bwo;
  __bf16* out;
  char* ws;
};

#define SBAR() __builtin_amdgcn_sched_barrier(0)
#define MEMCLOB() asm volatile("" ::: "memory")
#define LDS_BARRIER() asm volatile("s_waitcnt lgkmcnt(0)\n\ts_barrier" ::: "memory")
#define VM_LDS_BARRIER(N) asm volatile("s_waitcnt vmcnt(" #N ") lgkmcnt(0)\n\ts_barrier" ::: "memory")
#define VMWAIT(N) asm volatile("s_waitcnt vmcnt(" #N ")" ::: "memory")

DI unsigned pack2(float lo, float hi) { bf16x2 v = {(__bf16)lo, (__bf16)hi}; return __builtin_bit_cast(unsigned, v); }
DI bf16x8 as_bf8(u32x4 v) { return __builtin_bit_cast(bf16x8, v); }
DI f32x4 mfma16(bf16x8 a, bf16x8 b, f32x4 c) { return __builtin_amdgcn_mfma_f32_16x16x32_bf16(a, b, c, 0, 0, 0); }
DI f32x4 mfma16k16(u32x2 a, u32x2 b, f32x4 c) { return __builtin_amdgcn_mfma_f32_16x16x16bf16_1k(__builtin_bit_cast(s16x4, a), __builtin_bit_cast(s16x4, b), c, 0, 0, 0); }
DI f32x16 mfma32(bf16x8 a, bf16x8 b, f32x16 c) { return __builtin_amdgcn_mfma_f32_32x32x16_bf16(a, b, c, 0, 0, 0); }
DI f32x4 smfma(bf16x8 a, bf16x16 b, f32x4 c, int idx) { return __builtin_amdgcn_smfmac_f32_16x16x64_bf16(a, b, c, idx, 0, 0); }
DI s16x4 trread(lptr p) { return __builtin_amdgcn_ds_read_tr16_b64_v4i16((LAS s16x4*)p); }
DI bf16x8 tr2(lptr p0, lptr p1) { s16x4 a = trread(p0), b = trread(p1); return __builtin_bit_cast(bf16x8, __builtin_shufflevector(a, b, 0, 1, 2, 3, 4, 5, 6, 7)); }
DI bf16x16 cat16(bf16x8 a, bf16x8 b) { return __builtin_shufflevector(a, b, 0, 1, 2, 3, 4, 5, 6, 7, 8, 9, 10, 11, 12, 13, 14, 15); }
DI u32x4 lds_ld16(lptr p) { return *(LAS u32x4*)p; }
DI void lds_st16(lptr p, u32x4 v) { *(LAS u32x4*)p = v; }
DI void lds_st8(lptr p, u32x2 v) { *(LAS u32x2*)p = v; }
DI float fu(unsigned u) { return __builtin_bit_cast(float, u); }
DI unsigned uf(float f) { return __builtin_bit_cast(unsigned, f); }
DI float swapadd32(float a, float b) { auto r = __builtin_amdgcn_permlane32_swap(uf(a), uf(b), false, false); return fu(r[0]) + fu(r[1]); }
DI float xmax32(float a) { auto r = __builtin_amdgcn_permlane32_swap(uf(a), uf(a), false, false); return __builtin_fmaxf(fu(r[0]), fu(r[1])); }
DI float xadd32(float a) { auto r = __builtin_amdgcn_permlane32_swap(uf(a), uf(a), false, false); return fu(r[0]) + fu(r[1]); }
DI float xmax16(float a) { auto r = __builtin_amdgcn_permlane16_swap(uf(a), uf(a), false, false); return __builtin_fmaxf(fu(r[0]), fu(r[1])); }
DI float xadd16(float a) { auto r = __builtin_amdgcn_permlane16_swap(uf(a), uf(a), false, false); return fu(r[0]) + fu(r[1]); }
DI float max3f(float a, float b, float c) { return __builtin_fmaxf(__builtin_fmaxf(a, b), c); }
DI float max16(const f32x16& v) {
  float a = max3f(v[0], v[1], v[2]), b = max3f(v[3], v[4], v[5]);
  a = max3f(a, v[6], v[7]); b = max3f(b, v[8], v[9]); a = max3f(a, v[10], v[11]); b = max3f(b, v[12], v[13]);
  return __builtin_fmaxf(max3f(a, v[14], v[15]), b);
}
DI unsigned umax(unsigned a, unsigned b) { return a > b ? a : b; }
DI unsigned umax4(u32x4 v) { return umax(umax(umax(v.x, v.y), v.z), v.w); }
DI float ex2(float x) { return __builtin_amdgcn_exp2f(x); }
DI int fresh_lane() { int l; asm volatile("v_mbcnt_lo_u32_b32 %0, -1, 0\n\tv_mbcnt_hi_u32_b32 %0, -1, %0" : "=v"(l)); return l; }
DI int rfl(int x) { return __builtin_amdgcn_readfirstlane(x); }

DI u32x4 gld16(rsrc_t r, unsigned off) { return __builtin_amdgcn_raw_buffer_load_b128(r, off, 0, SC1); }
DI u32x2 gld8(rsrc_t r, unsigned off) { return __builtin_amdgcn_raw_buffer_load_b64(r, off, 0, SC1); }
DI unsigned gld4(rsrc_t r, unsigned off) { return __builtin_amdgcn_raw_buffer_load_b32(r, off, 0, SC1); }
DI void gst16(rsrc_t r, unsigned off, u32x4 v) { __builtin_amdgcn_raw_buffer_store_b128(v, r, off, 0, SC1); }
DI void gst8(rsrc_t r, unsigned off, u32x2 v) { __builtin_amdgcn_raw_buffer_store_b64(v, r, off, 0, SC1); }
DI void gst4(rsrc_t r, unsigned off, unsigned v) { __builtin_amdgcn_raw_buffer_store_b32(v, r, off, 0, SC1); }
DI void dma16(const void* base, unsigned voff, unsigned ldsdst) {
  unsigned keep;
  asm volatile("s_mov_b32 %0, m0\n\ts_mov_b32 m0, %3\n\ts_nop 0\n\tglobal_load_lds_dwordx4 %1, %2\n\ts_mov_b32 m0, %0" : "=&s"(keep) : "v"(voff), "s"(base), "s"(ldsdst) : "memory");
}
DI void dma16_sc1(const void* base, unsigned voff, unsigned ldsdst) {
  unsigned keep;
  asm volatile("s_mov_b32 %0, m0\n\ts_mov_b32 m0, %3\n\ts_nop 0\n\tglobal_load_lds_dwordx4 %1, %2 sc1\n\ts_mov_b32 m0, %0" : "=&s"(keep) : "v"(voff), "s"(base), "s"(ldsdst) : "memory");
}
DI void delay32(int n) {
  int c = n;
  asm volatile("1:\n\ts_sub_u32 %0, %0, 1\n\ts_cmp_lg_u32 %0, 0\n\ts_cbranch_scc1 1b" : "+s"(c) :: "scc", "memory");
}
constexpr int SPIN_CAP = 40000;
DI bool spin_fail(int& spins, rsrc_t ws) {
  ++spins;
  if (spins > SPIN_CAP) { gst4(ws, F_TMO, 1u); return true; }
  if ((spins & 63) == 0) { MEMCLOB(); return rfl((int)gld4(ws, F_TMO)) == 1; }
  return false;
}
DI int wait_ge(rsrc_t ws, unsigned off, int want, int sleep_units) {
  int spins = 0, v;
  for (;;) {
    MEMCLOB();
    v = (int)gld4(ws, off);
    if (__all(v >= want)) break;
    if (spin_fail(spins, ws)) break;
    if (sleep_units >= 8) __builtin_amdgcn_s_sleep(8); else if (sleep_units >= 2) __builtin_amdgcn_s_sleep(2);
  }
  MEMCLOB();
  return rfl(v);
}
struct Tag { unsigned lo, hi; };
DI int wait_tag(rsrc_t ws, unsigned off, Tag t) {
  u32x2 v;
  for (int spins = 0; spins < SPIN_CAP; ++spins) {
    MEMCLOB();
    v = gld8(ws, off);
    if (__all(v.y == t.hi && v.x - t.lo < 128u)) break;
    __builtin_amdgcn_s_sleep(2);
  }
  MEMCLOB();
  return rfl((int)(v.x - t.lo));
}
DI unsigned physx(unsigned off, unsigned sel13) { return ((off >> 13) << 14) | sel13 | (off & 8191u); }
DI int crow(int r, int hh) { return (r & 3) + 8 * (r >> 2) + 4 * hh; }

constexpr int H_WV = 0, H_MEM = 131072, H_X = H_MEM + 24576;
constexpr float C_A = 0.08838834764831845f * 1.4426950408889634f;

#define HSTR2(x) #x
#define HSTR(x) HSTR2(x)
DI void head_node(const Params& p, rsrc_t ws, lptr lds, int h, int a, Tag tag) {
  const int tid = threadIdx.x, lane = tid & 63, w = rfl(tid >> 6);
  const int gb = lane >> 4, n = lane & 15, q4 = n >> 2, pp = n & 3, hi = lane >> 5;
  const unsigned lds0 = (unsigned)(size_t)lds;
  unsigned qkv = (unsigned)(((32 * w + gb) * 4096 + h * 128) * 2 + (n ^ (gb << 1)) * 16);
  unsigned oe = (unsigned)(((h * 128 + 16 * w + hi) * 512 + 256 * a) * 2 + ((lane & 31) ^ ((hi | ((w & 1) << 2)) << 1)) * 16);
  const int phq = (gb & 1) | ((q4 >> 1) << 1) | ((q4 & 1) << 2), rowq = 16 * (q4 & 1) + 2 * (q4 >> 1) + 4 * (gb >> 1) + (gb & 1);
  const int pho = (q4 & 1) | ((q4 >> 1) << 1) | ((gb & 1) << 2), rowo = 16 * (gb & 1) + 8 * (gb >> 1) + 4 * (q4 >> 1) + (q4 & 1);
  unsigned trq = lds0 + rowq * 256 + ((4 * w + (pp >> 1)) ^ (phq << 1)) * 16 + 8 * (pp & 1);
  unsigned tro = lds0 + rowo * 512 + ((8 * w + (pp >> 1)) ^ (pho << 1)) * 16 + 8 * (pp & 1);
  const int mrow = 2 * (n >> 2) + (n & 1), mhalf = (n >> 1) & 1;
  unsigned m0o = (unsigned)((mrow * 512 + 64 * w + 4 * gb + 2 * mhalf) * 2);
  const unsigned l16 = (unsigned)lane * 16;
  const int idx = mhalf ? 0xEEEE : 0x4444;
  const int node = 2 * h + a;
  const unsigned pst = physx((unsigned)((4 * (8 * (4 * a + w) + 2 * (n >> 2) + (gb >> 1)) + 2 * (gb & 1) + mhalf) * 1024 + h * 32 + (n & 1) * 16), 0);
  const unsigned pld = physx((unsigned)((4 * node + w) * 1024 + (8 * gb + (n & 7)) * 32 + (n >> 3) * 16), 0);
  const unsigned mst = (lane & 0x19) == 0 ? MEM_PHYS + (unsigned)(node * 128 + w * 32 + (n >> 2) * 16 + (gb >> 1) * 8 + mhalf * 4) : 0x80000000u;
  const unsigned mwr = lds0 + H_MEM + w * 2048 + l16;
  const unsigned xwr = lds0 + H_X + (4 * w + (n >> 3)) * 256 + gb * 64 + (n & 7) * 8;
  const unsigned xrd = lds0 + H_X + (gb >> 1) * 256 + pp * 64 + (4 * (gb & 1) + q4) * 8;
  const unsigned owr = lds0 + H_X + (w >> 1) * 1024 + (2 * (w & 1) + (gb >> 1)) * 256 + ((n & ~2) + 2 * (gb & 1)) * 16 + mhalf * 8;
  const unsigned kvp = a == 0 ? (unsigned)(((w * 4 + gb) * 8 + mrow) * 16 + mhalf * 8) : 2048u + (unsigned)(((16 * w + n) * 4 + gb) * 8);
  const unsigned wvrd = lds0 + H_WV + w * 32768 + l16, memrd = lds0 + H_MEM + l16;
  const u32x4 ones = {0x3F803F80u, 0x3F803F80u, 0x3F803F80u, 0x3F803F80u};
  const u32x2 onesm = {0x3F803F80u, 0u};
  u32x4 selb;
#pragma unroll
  for (int j = 0; j < 4; ++j) selb[j] = n == 2 * j ? 0x3F80u : (n == 2 * j + 1 ? 0x3F800000u : 0u);
  const int haw = h | (a << 8) | (w << 16);
  const unsigned long long tag64 = ((unsigned long long)tag.hi << 32) | tag.lo;
  static_assert(WS_BYTES == 68 * MB, "the head asm carries this size");
  (void)ws;
  asm volatile(
      "s_waitcnt vmcnt(0) lgkmcnt(0)\n\t"
      "s_and_b32 s92, %[haw], 0xff\n\t"
      "s_bfe_u32 s93, %[haw], 0x80008\n\t"
      "s_lshr_b32 s94, %[haw], 16\n\t"
      "s_mov_b32 s64, m0\n\t"
      "s_memtime s[32:33]\n\t"
      "s_memrealtime s[34:35]\n\t"
      "s_waitcnt lgkmcnt(0)\n\t"
      "s_mov_b32 s36, s32\n\t"
      "s_mov_b32 s37, s34\n\t"
      "s_mov_b32 s28, s32\n\t"
      "s_mov_b32 s29, s34\n\t"
      "s_mov_b64 s[96:97], %[wsp]\n\t"
      "s_and_b32 s97, s97, 0xffff\n\t"
      "s_mov_b32 s99, 0x20000\n\t"
      "s_lshl_b32 s65, s94, 13\n\t"
      "s_mov_b32 s49, 0\n\t"
      "v_xor_b32 v160, 0x80, %[qkv]\n\t"
      "v_xor_b32 v161, 64, %[oe]\n\t"
      "global_load_dword v180, %[m0o], %[mem0] offset:0\n\t"
      "global_load_dword v181, %[m0o], %[mem0] offset:32\n\t"
      "global_load_dword v182, %[m0o], %[mem0] offset:64\n\t"
      "global_load_dword v183, %[m0o], %[mem0] offset:96\n\t"
      "global_load_dword v184, %[m0o], %[mem0] offset:512\n\t"
      "global_load_dword v185, %[m0o], %[mem0] offset:544\n\t"
      "global_load_dword v186, %[m0o], %[mem0] offset:576\n\t"
      "global_load_dword v187, %[m0o], %[mem0] offset:608\n\t"
      "s_mov_b64 s[60:61], %[wq]\n\t"
      "s_add_u32 m0, s65, 0\n\t"
      "s_add_u32 s62, s60, 0\n\t"
      "s_addc_u32 s63, s61, 0\n\t"
      "global_load_lds_dwordx4 %[qkv], s[62:63]\n\t"
      "s_add_u32 m0, s65, 1024\n\t"
      "s_add_u32 s62, s60, 32768\n\t"
      "s_addc_u32 s63, s61, 0\n\t"
      "global_load_lds_dwordx4 %[qkv], s[62:63]\n\t"
      "s_add_u32 m0, s65, 2048\n\t"
      "s_add_u32 s62, s60, 65536\n\t"
      "s_addc_u32 s63, s61, 0\n\t"
      "global_load_lds_dwordx4 %[qkv], s[62:63]\n\t"
      "s_add_u32 m0, s65, 3072\n\t"
      "s_add_u32 s62, s60, 98304\n\t"
      "s_addc_u32 s63, s61, 0\n\t"
      "global_load_lds_dwordx4 %[qkv], s[62:63]\n\t"
      "s_add_u32 m0, s65, 4096\n\t"
      "s_add_u32 s62, s60, 131072\n\t"
      "s_addc_u32 s63, s61, 0\n\t"
      "global_load_lds_dwordx4 v160, s[62:63]\n\t"
      "s_add_u32 m0, s65, 5120\n\t"
      "s_add_u32 s62, s60, 163840\n\t"
      "s_addc_u32 s63, s61, 0\n\t"
      "global_load_lds_dwordx4 v160, s[62:63]\n\t"
      "s_add_u32 m0, s65, 6144\n\t"
      "s_add_u32 s62, s60, 196608\n\t"
      "s_addc_u32 s63, s61, 0\n\t"
      "global_load_lds_dwordx4 v160, s[62:63]\n\t"
      "s_add_u32 m0, s65, 7168\n\t"
      "s_add_u32 s62, s60, 229376\n\t"
      "s_addc_u32 s63, s61, 0\n\t"
      "global_load_lds_dwordx4 v160, s[62:63]\n\t"
      "s_add_u32 m0, s65, 32768\n\t"
      "s_add_u32 s62, s60, 1048576\n\t"
      "s_addc_u32 s63, s61, 0\n\t"
      "global_load_lds_dwordx4 %[qkv], s[62:63]\n\t"
      "s_add_u32 m0, s65, 33792\n\t"
      "s_add_u32 s62, s60, 1081344\n\t"
      "s_addc_u32 s63, s61, 0\n\t"
      "global_load_lds_dwordx4 %[qkv], s[62:63]\n\t"
      "s_add_u32 m0, s65, 34816\n\t"
      "s_add_u32 s62, s60, 1114112\n\t"
      "s_addc_u32 s63, s61, 0\n\t"
      "global_load_lds_dwordx4 %[qkv], s[62:63]\n\t"
      "s_add_u32 m0, s65, 35840\n\t"
      "s_add_u32 s62, s60, 1146880\n\t"
      "s_addc_u32 s63, s61, 0\n\t"
      "global_load_lds_dwordx4 %[qkv], s[62:63]\n\t"
      "s_add_u32 m0, s65, 36864\n\t"
      "s_add_u32 s62, s60, 1179648\n\t"
      "s_addc_u32 s63, s61, 0\n\t"
      "global_load_lds_dwordx4 v160, s[62:63]\n\t"
      "s_add_u32 m0, s65, 37888\n\t"
      "s_add_u32 s62, s60, 1212416\n\t"
      "s_addc_u32 s63, s61, 0\n\t"
      "global_load_lds_dwordx4 v160, s[62:63]\n\t"
      "s_add_u32 m0, s65, 38912\n\t"
      "s_add_u32 s62, s60, 1245184\n\t"
      "s_addc_u32 s63, s61, 0\n\t"
      "global_load_lds_dwordx4 v160, s[62:63]\n\t"
      "s_add_u32 m0, s65, 39936\n\t"
      "s_add_u32 s62, s60, 1277952\n\t"
      "s_addc_u32 s63, s61, 0\n\t"
      "global_load_lds_dwordx4 v160, s[62:63]\n\t"
      "s_add_u32 m0, s65, 65536\n\t"
      "s_add_u32 s62, s60, 2097152\n\t"
      "s_addc_u32 s63, s61, 0\n\t"
      "global_load_lds_dwordx4 %[qkv], s[62:63]\n\t"
      "s_add_u32 m0, s65, 66560\n\t"
      "s_add_u32 s62, s60, 2129920\n\t"
      "s_addc_u32 s63, s61, 0\n\t"
      "global_load_lds_dwordx4 %[qkv], s[62:63]\n\t"
      "s_add_u32 m0, s65, 67584\n\t"
      "s_add_u32 s62, s60, 2162688\n\t"
      "s_addc_u32 s63, s61, 0\n\t"
      "global_load_lds_dwordx4 %[qkv], s[62:63]\n\t"
      "s_add_u32 m0, s65, 68608\n\t"
      "s_add_u32 s62, s60, 2195456\n\t"
      "s_addc_u32 s63, s61, 0\n\t"
      "global_load_lds_dwordx4 %[qkv], s[62:63]\n\t"
      "s_add_u32 m0, s65, 69632\n\t"
      "s_add_u32 s62, s60, 2228224\n\t"
      "s_addc_u32 s63, s61, 0\n\t"
      "global_load_lds_dwordx4 v160, s[62:63]\n\t"
      "s_add_u32 m0, s65, 70656\n\t"
      "s_add_u32 s62, s60, 2260992\n\t"
      "s_addc_u32 s63, s61, 0\n\t"
      "global_load_lds_dwordx4 v160, s[62:63]\n\t"
      "s_add_u32 m0, s65, 71680\n\t"
      "s_add_u32 s62, s60, 2293760\n\t"
      "s_addc_u32 s63, s61, 0\n\t"
      "global_load_lds_dwordx4 v160, s[62:63]\n\t"
      "s_add_u32 m0, s65, 72704\n\t"
      "s_add_u32 s62, s60, 2326528\n\t"
      "s_addc_u32 s63, s61, 0\n\t"
      "global_load_lds_dwordx4 v160, s[62:63]\n\t"
      "s_add_u32 m0, s65, 98304\n\t"
      "s_add_u32 s62, s60, 3145728\n\t"
      "s_addc_u32 s63, s61, 0\n\t"
      "global_load_lds_dwordx4 %[qkv], s[62:63]\n\t"
      "s_add_u32 m0, s65, 99328\n\t"
      "s_add_u32 s62, s60, 3178496\n\t"
      "s_addc_u32 s63, s61, 0\n\t"
      "global_load_lds_dwordx4 %[qkv], s[62:63]\n\t"
      "s_add_u32 m0, s65, 100352\n\t"
      "s_add_u32 s62, s60, 3211264\n\t"
      "s_addc_u32 s63, s61, 0\n\t"
      "global_load_lds_dwordx4 %[qkv], s[62:63]\n\t"
      "s_add_u32 m0, s65, 101376\n\t"
      "s_add_u32 s62, s60, 3244032\n\t"
      "s_addc_u32 s63, s61, 0\n\t"
      "global_load_lds_dwordx4 %[qkv], s[62:63]\n\t"
      "s_add_u32 m0, s65, 102400\n\t"
      "s_add_u32 s62, s60, 3276800\n\t"
      "s_addc_u32 s63, s61, 0\n\t"
      "global_load_lds_dwordx4 v160, s[62:63]\n\t"
      "s_add_u32 m0, s65, 103424\n\t"
      "s_add_u32 s62, s60, 3309568\n\t"
      "s_addc_u32 s63, s61, 0\n\t"
      "global_load_lds_dwordx4 v160, s[62:63]\n\t"
      "s_add_u32 m0, s65, 104448\n\t"
      "s_add_u32 s62, s60, 3342336\n\t"
      "s_addc_u32 s63, s61, 0\n\t"
      "global_load_lds_dwordx4 v160, s[62:63]\n\t"
      "s_add_u32 m0, s65, 105472\n\t"
      "s_add_u32 s62, s60, 3375104\n\t"
      "s_addc_u32 s63, s61, 0\n\t"
      "global_load_lds_dwordx4 v160, s[62:63]\n\t"
      "s_waitcnt vmcnt(24) lgkmcnt(0)\n\t"
      "s_barrier\n\t"
      "s_mov_b64 s[60:61], %[wk]\n\t"
      "s_add_u32 m0, s65, 131072\n\t"
      "s_add_u32 s62, s60, 0\n\t"
      "s_addc_u32 s63, s61, 0\n\t"
      "global_load_lds_dwordx4 %[qkv], s[62:63]\n\t"
      "s_add_u32 m0, s65, 132096\n\t"
      "s_add_u32 s62, s60, 32768\n\t"
      "s_addc_u32 s63, s61, 0\n\t"
      "global_load_lds_dwordx4 %[qkv], s[62:63]\n\t"
      "s_add_u32 m0, s65, 133120\n\t"
      "s_add_u32 s62, s60, 65536\n\t"
      "s_addc_u32 s63, s61, 0\n\t"
      "global_load_lds_dwordx4 %[qkv], s[62:63]\n\t"
      "s_add_u32 m0, s65, 134144\n\t"
      "s_add_u32 s62, s60, 98304\n\t"
      "s_addc_u32 s63, s61, 0\n\t"
      "global_load_lds_dwordx4 %[qkv], s[62:63]\n\t"
      "s_add_u32 m0, s65, 135168\n\t"
      "s_add_u32 s62, s60, 131072\n\t"
      "s_addc_u32 s63, s61, 0\n\t"
      "global_load_lds_dwordx4 v160, s[62:63]\n\t"
      "s_add_u32 m0, s65, 136192\n\t"
      "s_add_u32 s62, s60, 163840\n\t"
      "s_addc_u32 s63, s61, 0\n\t"
      "global_load_lds_dwordx4 v160, s[62:63]\n\t"
      "s_add_u32 m0, s65, 137216\n\t"
      "s_add_u32 s62, s60, 196608\n\t"
      "s_addc_u32 s63, s61, 0\n\t"
      "global_load_lds_dwordx4 v160, s[62:63]\n\t"
      "s_add_u32 m0, s65, 138240\n\t"
      "s_add_u32 s62, s60, 229376\n\t"
      "s_addc_u32 s63, s61, 0\n\t"
      "global_load_lds_dwordx4 v160, s[62:63]\n\t"
      "v_add_u32 v162, 0, %[trq]\n\t"
      "v_xor_b32 v163, 32, v162\n\t"
      "ds_read_b64_tr_b16 a[0:1], v162 offset:0\n\t"
      "ds_read_b64_tr_b16 a[2:3], v162 offset:8192\n\t"
      "ds_read_b64_tr_b16 a[4:5], v162 offset:2048\n\t"
      "ds_read_b64_tr_b16 a[6:7], v162 offset:10240\n\t"
      "ds_read_b64_tr_b16 a[64:65], v163 offset:0\n\t"
      "ds_read_b64_tr_b16 a[66:67], v163 offset:8192\n\t"
      "ds_read_b64_tr_b16 a[68:69], v163 offset:2048\n\t"
      "ds_read_b64_tr_b16 a[70:71], v163 offset:10240\n\t"
      "ds_read_b64_tr_b16 a[8:9], v162 offset:16384\n\t"
      "ds_read_b64_tr_b16 a[10:11], v162 offset:24576\n\t"
      "ds_read_b64_tr_b16 a[12:13], v162 offset:18432\n\t"
      "ds_read_b64_tr_b16 a[14:15], v162 offset:26624\n\t"
      "ds_read_b64_tr_b16 a[72:73], v163 offset:16384\n\t"
      "ds_read_b64_tr_b16 a[74:75], v163 offset:24576\n\t"
      "ds_read_b64_tr_b16 a[76:77], v163 offset:18432\n\t"
      "ds_read_b64_tr_b16 a[78:79], v163 offset:26624\n\t"
      "s_waitcnt vmcnt(24) lgkmcnt(0)\n\t"
      "s_barrier\n\t"
      "s_add_u32 m0, s65, 0\n\t"
      "s_add_u32 s62, s60, 1048576\n\t"
      "s_addc_u32 s63, s61, 0\n\t"
      "global_load_lds_dwordx4 %[qkv], s[62:63]\n\t"
      "s_add_u32 m0, s65, 1024\n\t"
      "s_add_u32 s62, s60, 1081344\n\t"
      "s_addc_u32 s63, s61, 0\n\t"
      "global_load_lds_dwordx4 %[qkv], s[62:63]\n\t"
      "s_add_u32 m0, s65, 2048\n\t"
      "s_add_u32 s62, s60, 1114112\n\t"
      "s_addc_u32 s63, s61, 0\n\t"
      "global_load_lds_dwordx4 %[qkv], s[62:63]\n\t"
      "s_add_u32 m0, s65, 3072\n\t"
      "s_add_u32 s62, s60, 1146880\n\t"
      "s_addc_u32 s63, s61, 0\n\t"
      "global_load_lds_dwordx4 %[qkv], s[62:63]\n\t"
      "s_add_u32 m0, s65, 4096\n\t"
      "s_add_u32 s62, s60, 1179648\n\t"
      "s_addc_u32 s63, s61, 0\n\t"
      "global_load_lds_dwordx4 v160, s[62:63]\n\t"
      "s_add_u32 m0, s65, 5120\n\t"
      "s_add_u32 s62, s60, 1212416\n\t"
      "s_addc_u32 s63, s61, 0\n\t"
      "global_load_lds_dwordx4 v160, s[62:63]\n\t"
      "s_add_u32 m0, s65, 6144\n\t"
      "s_add_u32 s62, s60, 1245184\n\t"
      "s_addc_u32 s63, s61, 0\n\t"
      "global_load_lds_dwordx4 v160, s[62:63]\n\t"
      "s_add_u32 m0, s65, 7168\n\t"
      "s_add_u32 s62, s60, 1277952\n\t"
      "s_addc_u32 s63, s61, 0\n\t"
      "global_load_lds_dwordx4 v160, s[62:63]\n\t"
      "v_add_u32 v162, 32768, %[trq]\n\t"
      "v_xor_b32 v163, 32, v162\n\t"
      "ds_read_b64_tr_b16 a[16:17], v162 offset:0\n\t"
      "ds_read_b64_tr_b16 a[18:19], v162 offset:8192\n\t"
      "ds_read_b64_tr_b16 a[20:21], v162 offset:2048\n\t"
      "ds_read_b64_tr_b16 a[22:23], v162 offset:10240\n\t"
      "ds_read_b64_tr_b16 a[80:81], v163 offset:0\n\t"
      "ds_read_b64_tr_b16 a[82:83], v163 offset:8192\n\t"
      "ds_read_b64_tr_b16 a[84:85], v163 offset:2048\n\t"
      "ds_read_b64_tr_b16 a[86:87], v163 offset:10240\n\t"
      "ds_read_b64_tr_b16 a[24:25], v162 offset:16384\n\t"
      "ds_read_b64_tr_b16 a[26:27], v162 offset:24576\n\t"
      "ds_read_b64_tr_b16 a[28:29], v162 offset:18432\n\t"
      "ds_read_b64_tr_b16 a[30:31], v162 offset:26624\n\t"
      "ds_read_b64_tr_b16 a[88:89], v163 offset:16384\n\t"
      "ds_read_b64_tr_b16 a[90:91], v163 offset:24576\n\t"
      "ds_read_b64_tr_b16 a[92:93], v163 offset:18432\n\t"
      "ds_read_b64_tr_b16 a[94:95], v163 offset:26624\n\t"
      "s_waitcnt vmcnt(24) lgkmcnt(0)\n\t"
      "s_barrier\n\t"
      "s_add_u32 m0, s65, 32768\n\t"
      "s_add_u32 s62, s60, 2097152\n\t"
      "s_addc_u32 s63, s61, 0\n\t"
      "global_load_lds_dwordx4 %[qkv], s[62:63]\n\t"
      "s_add_u32 m0, s65, 33792\n\t"
      "s_add_u32 s62, s60, 2129920\n\t"
      "s_addc_u32 s63, s61, 0\n\t"
      "global_load_lds_dwordx4 %[qkv], s[62:63]\n\t"
      "s_add_u32 m0, s65, 34816\n\t"
      "s_add_u32 s62, s60, 2162688\n\t"
      "s_addc_u32 s63, s61, 0\n\t"
      "global_load_lds_dwordx4 %[qkv], s[62:63]\n\t"
      "s_add_u32 m0, s65, 35840\n\t"
      "s_add_u32 s62, s60, 2195456\n\t"
      "s_addc_u32 s63, s61, 0\n\t"
      "global_load_lds_dwordx4 %[qkv], s[62:63]\n\t"
      "s_add_u32 m0, s65, 36864\n\t"
      "s_add_u32 s62, s60, 2228224\n\t"
      "s_addc_u32 s63, s61, 0\n\t"
      "global_load_lds_dwordx4 v160, s[62:63]\n\t"
      "s_add_u32 m0, s65, 37888\n\t"
      "s_add_u32 s62, s60, 2260992\n\t"
      "s_addc_u32 s63, s61, 0\n\t"
      "global_load_lds_dwordx4 v160, s[62:63]\n\t"
      "s_add_u32 m0, s65, 38912\n\t"
      "s_add_u32 s62, s60, 2293760\n\t"
      "s_addc_u32 s63, s61, 0\n\t"
      "global_load_lds_dwordx4 v160, s[62:63]\n\t"
      "s_add_u32 m0, s65, 39936\n\t"
      "s_add_u32 s62, s60, 2326528\n\t"
      "s_addc_u32 s63, s61, 0\n\t"
      "global_load_lds_dwordx4 v160, s[62:63]\n\t"
      "v_add_u32 v162, 65536, %[trq]\n\t"
      "v_xor_b32 v163, 32, v162\n\t"
      "ds_read_b64_tr_b16 a[32:33], v162 offset:0\n\t"
      "ds_read_b64_tr_b16 a[34:35], v162 offset:8192\n\t"
      "ds_read_b64_tr_b16 a[36:37], v162 offset:2048\n\t"
      "ds_read_b64_tr_b16 a[38:39], v162 offset:10240\n\t"
      "ds_read_b64_tr_b16 a[96:97], v163 offset:0\n\t"
      "ds_read_b64_tr_b16 a[98:99], v163 offset:8192\n\t"
      "ds_read_b64_tr_b16 a[100:101], v163 offset:2048\n\t"
      "ds_read_b64_tr_b16 a[102:103], v163 offset:10240\n\t"
      "ds_read_b64_tr_b16 a[40:41], v162 offset:16384\n\t"
      "ds_read_b64_tr_b16 a[42:43], v162 offset:24576\n\t"
      "ds_read_b64_tr_b16 a[44:45], v162 offset:18432\n\t"
      "ds_read_b64_tr_b16 a[46:47], v162 offset:26624\n\t"
      "ds_read_b64_tr_b16 a[104:105], v163 offset:16384\n\t"
      "ds_read_b64_tr_b16 a[106:107], v163 offset:24576\n\t"
      "ds_read_b64_tr_b16 a[108:109], v163 offset:18432\n\t"
      "ds_read_b64_tr_b16 a[110:111], v163 offset:26624\n\t"
      "s_waitcnt vmcnt(24) lgkmcnt(0)\n\t"
      "s_barrier\n\t"
      "s_add_u32 m0, s65, 65536\n\t"
      "s_add_u32 s62, s60, 3145728\n\t"
      "s_addc_u32 s63, s61, 0\n\t"
      "global_load_lds_dwordx4 %[qkv], s[62:63]\n\t"
      "s_add_u32 m0, s65, 66560\n\t"
      "s_add_u32 s62, s60, 3178496\n\t"
      "s_addc_u32 s63, s61, 0\n\t"
      "global_load_lds_dwordx4 %[qkv], s[62:63]\n\t"
      "s_add_u32 m0, s65, 67584\n\t"
      "s_add_u32 s62, s60, 3211264\n\t"
      "s_addc_u32 s63, s61, 0\n\t"
      "global_load_lds_dwordx4 %[qkv], s[62:63]\n\t"
      "s_add_u32 m0, s65, 68608\n\t"
      "s_add_u32 s62, s60, 3244032\n\t"
      "s_addc_u32 s63, s61, 0\n\t"
      "global_load_lds_dwordx4 %[qkv], s[62:63]\n\t"
      "s_add_u32 m0, s65, 69632\n\t"
      "s_add_u32 s62, s60, 3276800\n\t"
      "s_addc_u32 s63, s61, 0\n\t"
      "global_load_lds_dwordx4 v160, s[62:63]\n\t"
      "s_add_u32 m0, s65, 70656\n\t"
      "s_add_u32 s62, s60, 3309568\n\t"
      "s_addc_u32 s63, s61, 0\n\t"
      "global_load_lds_dwordx4 v160, s[62:63]\n\t"
      "s_add_u32 m0, s65, 71680\n\t"
      "s_add_u32 s62, s60, 3342336\n\t"
      "s_addc_u32 s63, s61, 0\n\t"
      "global_load_lds_dwordx4 v160, s[62:63]\n\t"
      "s_add_u32 m0, s65, 72704\n\t"
      "s_add_u32 s62, s60, 3375104\n\t"
      "s_addc_u32 s63, s61, 0\n\t"
      "global_load_lds_dwordx4 v160, s[62:63]\n\t"
      "v_add_u32 v162, 98304, %[trq]\n\t"
      "v_xor_b32 v163, 32, v162\n\t"
      "ds_read_b64_tr_b16 a[48:49], v162 offset:0\n\t"
      "ds_read_b64_tr_b16 a[50:51], v162 offset:8192\n\t"
      "ds_read_b64_tr_b16 a[52:53], v162 offset:2048\n\t"
      "ds_read_b64_tr_b16 a[54:55], v162 offset:10240\n\t"
      "ds_read_b64_tr_b16 a[112:113], v163 offset:0\n\t"
      "ds_read_b64_tr_b16 a[114:115], v163 offset:8192\n\t"
      "ds_read_b64_tr_b16 a[116:117], v163 offset:2048\n\t"
      "ds_read_b64_tr_b16 a[118:119], v163 offset:10240\n\t"
      "ds_read_b64_tr_b16 a[56:57], v162 offset:16384\n\t"
      "ds_read_b64_tr_b16 a[58:59], v162 offset:24576\n\t"
      "ds_read_b64_tr_b16 a[60:61], v162 offset:18432\n\t"
      "ds_read_b64_tr_b16 a[62:63], v162 offset:26624\n\t"
      "ds_read_b64_tr_b16 a[120:121], v163 offset:16384\n\t"
      "ds_read_b64_tr_b16 a[122:123], v163 offset:24576\n\t"
      "ds_read_b64_tr_b16 a[124:125], v163 offset:18432\n\t"
      "ds_read_b64_tr_b16 a[126:127], v163 offset:26624\n\t"
      "s_waitcnt vmcnt(24) lgkmcnt(0)\n\t"
      "s_barrier\n\t"
      "s_mov_b64 s[60:61], %[wv]\n\t"
      "s_add_u32 m0, s65, 98304\n\t"
      "s_add_u32 s62, s60, 0\n\t"
      "s_addc_u32 s63, s61, 0\n\t"
      "global_load_lds_dwordx4 %[qkv], s[62:63]\n\t"
      "s_add_u32 m0, s65, 99328\n\t"
      "s_add_u32 s62, s60, 32768\n\t"
      "s_addc_u32 s63, s61, 0\n\t"
      "global_load_lds_dwordx4 %[qkv], s[62:63]\n\t"
      "s_add_u32 m0, s65, 100352\n\t"
      "s_add_u32 s62, s60, 65536\n\t"
      "s_addc_u32 s63, s61, 0\n\t"
      "global_load_lds_dwordx4 %[qkv], s[62:63]\n\t"
      "s_add_u32 m0, s65, 101376\n\t"
      "s_add_u32 s62, s60, 98304\n\t"
      "s_addc_u32 s63, s61, 0\n\t"
      "global_load_lds_dwordx4 %[qkv], s[62:63]\n\t"
      "s_add_u32 m0, s65, 102400\n\t"
      "s_add_u32 s62, s60, 131072\n\t"
      "s_addc_u32 s63, s61, 0\n\t"
      "global_load_lds_dwordx4 v160, s[62:63]\n\t"
      "s_add_u32 m0, s65, 103424\n\t"
      "s_add_u32 s62, s60, 163840\n\t"
      "s_addc_u32 s63, s61, 0\n\t"
      "global_load_lds_dwordx4 v160, s[62:63]\n\t"
      "s_add_u32 m0, s65, 104448\n\t"
      "s_add_u32 s62, s60, 196608\n\t"
      "s_addc_u32 s63, s61, 0\n\t"
      "global_load_lds_dwordx4 v160, s[62:63]\n\t"
      "s_add_u32 m0, s65, 105472\n\t"
      "s_add_u32 s62, s60, 229376\n\t"
      "s_addc_u32 s63, s61, 0\n\t"
      "global_load_lds_dwordx4 v160, s[62:63]\n\t"
      "v_add_u32 v162, 131072, %[trq]\n\t"
      "v_xor_b32 v163, 32, v162\n\t"
      "ds_read_b64_tr_b16 a[128:129], v162 offset:0\n\t"
      "ds_read_b64_tr_b16 a[130:131], v162 offset:8192\n\t"
      "ds_read_b64_tr_b16 a[132:133], v162 offset:2048\n\t"
      "ds_read_b64_tr_b16 a[134:135], v162 offset:10240\n\t"
      "ds_read_b64_tr_b16 a[192:193], v163 offset:0\n\t"
      "ds_read_b64_tr_b16 a[194:195], v163 offset:8192\n\t"
      "ds_read_b64_tr_b16 a[196:197], v163 offset:2048\n\t"
      "ds_read_b64_tr_b16 a[198:199], v163 offset:10240\n\t"
      "ds_read_b64_tr_b16 a[136:137], v162 offset:16384\n\t"
      "ds_read_b64_tr_b16 a[138:139], v162 offset:24576\n\t"
      "ds_read_b64_tr_b16 a[140:141], v162 offset:18432\n\t"
      "ds_read_b64_tr_b16 a[142:143], v162 offset:26624\n\t"
      "ds_read_b64_tr_b16 a[200:201], v163 offset:16384\n\t"
      "ds_read_b64_tr_b16 a[202:203], v163 offset:24576\n\t"
      "ds_read_b64_tr_b16 a[204:205], v163 offset:18432\n\t"
      "ds_read_b64_tr_b16 a[206:207], v163 offset:26624\n\t"
      "s_waitcnt vmcnt(24) lgkmcnt(0)\n\t"
      "s_barrier\n\t"
      "s_add_u32 m0, s65, 131072\n\t"
      "s_add_u32 s62, s60, 1048576\n\t"
      "s_addc_u32 s63, s61, 0\n\t"
      "global_load_lds_dwordx4 %[qkv], s[62:63]\n\t"
      "s_add_u32 m0, s65, 132096\n\t"
      "s_add_u32 s62, s60, 1081344\n\t"
      "s_addc_u32 s63, s61, 0\n\t"
      "global_load_lds_dwordx4 %[qkv], s[62:63]\n\t"
      "s_add_u32 m0, s65, 133120\n\t"
      "s_add_u32 s62, s60, 1114112\n\t"
      "s_addc_u32 s63, s61, 0\n\t"
      "global_load_lds_dwordx4 %[qkv], s[62:63]\n\t"
      "s_add_u32 m0, s65, 134144\n\t"
      "s_add_u32 s62, s60, 1146880\n\t"
      "s_addc_u32 s63, s61, 0\n\t"
      "global_load_lds_dwordx4 %[qkv], s[62:63]\n\t"
      "s_add_u32 m0, s65, 135168\n\t"
      "s_add_u32 s62, s60, 1179648\n\t"
      "s_addc_u32 s63, s61, 0\n\t"
      "global_load_lds_dwordx4 v160, s[62:63]\n\t"
      "s_add_u32 m0, s65, 136192\n\t"
      "s_add_u32 s62, s60, 1212416\n\t"
      "s_addc_u32 s63, s61, 0\n\t"
      "global_load_lds_dwordx4 v160, s[62:63]\n\t"
      "s_add_u32 m0, s65, 137216\n\t"
      "s_add_u32 s62, s60, 1245184\n\t"
      "s_addc_u32 s63, s61, 0\n\t"
      "global_load_lds_dwordx4 v160, s[62:63]\n\t"
      "s_add_u32 m0, s65, 138240\n\t"
      "s_add_u32 s62, s60, 1277952\n\t"
      "s_addc_u32 s63, s61, 0\n\t"
      "global_load_lds_dwordx4 v160, s[62:63]\n\t"
      "v_add_u32 v162, 0, %[trq]\n\t"
      "v_xor_b32 v163, 32, v162\n\t"
      "ds_read_b64_tr_b16 a[144:145], v162 offset:0\n\t"
      "ds_read_b64_tr_b16 a[146:147], v162 offset:8192\n\t"
      "ds_read_b64_tr_b16 a[148:149], v162 offset:2048\n\t"
      "ds_read_b64_tr_b16 a[150:151], v162 offset:10240\n\t"
      "ds_read_b64_tr_b16 a[208:209], v163 offset:0\n\t"
      "ds_read_b64_tr_b16 a[210:211], v163 offset:8192\n\t"
      "ds_read_b64_tr_b16 a[212:213], v163 offset:2048\n\t"
      "ds_read_b64_tr_b16 a[214:215], v163 offset:10240\n\t"
      "ds_read_b64_tr_b16 a[152:153], v162 offset:16384\n\t"
      "ds_read_b64_tr_b16 a[154:155], v162 offset:24576\n\t"
      "ds_read_b64_tr_b16 a[156:157], v162 offset:18432\n\t"
      "ds_read_b64_tr_b16 a[158:159], v162 offset:26624\n\t"
      "ds_read_b64_tr_b16 a[216:217], v163 offset:16384\n\t"
      "ds_read_b64_tr_b16 a[218:219], v163 offset:24576\n\t"
      "ds_read_b64_tr_b16 a[220:221], v163 offset:18432\n\t"
      "ds_read_b64_tr_b16 a[222:223], v163 offset:26624\n\t"
      "s_waitcnt vmcnt(24) lgkmcnt(0)\n\t"
      "s_barrier\n\t"
      "s_add_u32 m0, s65, 0\n\t"
      "s_add_u32 s62, s60, 2097152\n\t"
      "s_addc_u32 s63, s61, 0\n\t"
      "global_load_lds_dwordx4 %[qkv], s[62:63]\n\t"
      "s_add_u32 m0, s65, 1024\n\t"
      "s_add_u32 s62, s60, 2129920\n\t"
      "s_addc_u32 s63, s61, 0\n\t"
      "global_load_lds_dwordx4 %[qkv], s[62:63]\n\t"
      "s_add_u32 m0, s65, 2048\n\t"
      "s_add_u32 s62, s60, 2162688\n\t"
      "s_addc_u32 s63, s61, 0\n\t"
      "global_load_lds_dwordx4 %[qkv], s[62:63]\n\t"
      "s_add_u32 m0, s65, 3072\n\t"
      "s_add_u32 s62, s60, 2195456\n\t"
      "s_addc_u32 s63, s61, 0\n\t"
      "global_load_lds_dwordx4 %[qkv], s[62:63]\n\t"
      "s_add_u32 m0, s65, 4096\n\t"
      "s_add_u32 s62, s60, 2228224\n\t"
      "s_addc_u32 s63, s61, 0\n\t"
      "global_load_lds_dwordx4 v160, s[62:63]\n\t"
      "s_add_u32 m0, s65, 5120\n\t"
      "s_add_u32 s62, s60, 2260992\n\t"
      "s_addc_u32 s63, s61, 0\n\t"
      "global_load_lds_dwordx4 v160, s[62:63]\n\t"
      "s_add_u32 m0, s65, 6144\n\t"
      "s_add_u32 s62, s60, 2293760\n\t"
      "s_addc_u32 s63, s61, 0\n\t"
      "global_load_lds_dwordx4 v160, s[62:63]\n\t"
      "s_add_u32 m0, s65, 7168\n\t"
      "s_add_u32 s62, s60, 2326528\n\t"
      "s_addc_u32 s63, s61, 0\n\t"
      "global_load_lds_dwordx4 v160, s[62:63]\n\t"
      "v_add_u32 v162, 32768, %[trq]\n\t"
      "v_xor_b32 v163, 32, v162\n\t"
      "ds_read_b64_tr_b16 a[160:161], v162 offset:0\n\t"
      "ds_read_b64_tr_b16 a[162:163], v162 offset:8192\n\t"
      "ds_read_b64_tr_b16 a[164:165], v162 offset:2048\n\t"
      "ds_read_b64_tr_b16 a[166:167], v162 offset:10240\n\t"
      "ds_read_b64_tr_b16 a[224:225], v163 offset:0\n\t"
      "ds_read_b64_tr_b16 a[226:227], v163 offset:8192\n\t"
      "ds_read_b64_tr_b16 a[228:229], v163 offset:2048\n\t"
      "ds_read_b64_tr_b16 a[230:231], v163 offset:10240\n\t"
      "ds_read_b64_tr_b16 a[168:169], v162 offset:16384\n\t"
      "ds_read_b64_tr_b16 a[170:171], v162 offset:24576\n\t"
      "ds_read_b64_tr_b16 a[172:173], v162 offset:18432\n\t"
      "ds_read_b64_tr_b16 a[174:175], v162 offset:26624\n\t"
      "ds_read_b64_tr_b16 a[232:233], v163 offset:16384\n\t"
      "ds_read_b64_tr_b16 a[234:235], v163 offset:24576\n\t"
      "ds_read_b64_tr_b16 a[236:237], v163 offset:18432\n\t"
      "ds_read_b64_tr_b16 a[238:239], v163 offset:26624\n\t"
      "s_waitcnt vmcnt(24) lgkmcnt(0)\n\t"
      "s_barrier\n\t"
      "s_add_u32 m0, s65, 32768\n\t"
      "s_add_u32 s62, s60, 3145728\n\t"
      "s_addc_u32 s63, s61, 0\n\t"
      "global_load_lds_dwordx4 %[qkv], s[62:63]\n\t"
      "s_add_u32 m0, s65, 33792\n\t"
      "s_add_u32 s62, s60, 3178496\n\t"
      "s_addc_u32 s63, s61, 0\n\t"
      "global_load_lds_dwordx4 %[qkv], s[62:63]\n\t"
      "s_add_u32 m0, s65, 34816\n\t"
      "s_add_u32 s62, s60, 3211264\n\t"
      "s_addc_u32 s63, s61, 0\n\t"
      "global_load_lds_dwordx4 %[qkv], s[62:63]\n\t"
      "s_add_u32 m0, s65, 35840\n\t"
      "s_add_u32 s62, s60, 3244032\n\t"
      "s_addc_u32 s63, s61, 0\n\t"
      "global_load_lds_dwordx4 %[qkv], s[62:63]\n\t"
      "s_add_u32 m0, s65, 36864\n\t"
      "s_add_u32 s62, s60, 3276800\n\t"
      "s_addc_u32 s63, s61, 0\n\t"
      "global_load_lds_dwordx4 v160, s[62:63]\n\t"
      "s_add_u32 m0, s65, 37888\n\t"
      "s_add_u32 s62, s60, 3309568\n\t"
      "s_addc_u32 s63, s61, 0\n\t"
      "global_load_lds_dwordx4 v160, s[62:63]\n\t"
      "s_add_u32 m0, s65, 38912\n\t"
      "s_add_u32 s62, s60, 3342336\n\t"
      "s_addc_u32 s63, s61, 0\n\t"
      "global_load_lds_dwordx4 v160, s[62:63]\n\t"
      "s_add_u32 m0, s65, 39936\n\t"
      "s_add_u32 s62, s60, 3375104\n\t"
      "s_addc_u32 s63, s61, 0\n\t"
      "global_load_lds_dwordx4 v160, s[62:63]\n\t"
      "v_add_u32 v162, 65536, %[trq]\n\t"
      "v_xor_b32 v163, 32, v162\n\t"
      "ds_read_b64_tr_b16 a[176:177], v162 offset:0\n\t"
      "ds_read_b64_tr_b16 a[178:179], v162 offset:8192\n\t"
      "ds_read_b64_tr_b16 a[180:181], v162 offset:2048\n\t"
      "ds_read_b64_tr_b16 a[182:183], v162 offset:10240\n\t"
      "ds_read_b64_tr_b16 a[240:241], v163 offset:0\n\t"
      "ds_read_b64_tr_b16 a[242:243], v163 offset:8192\n\t"
      "ds_read_b64_tr_b16 a[244:245], v163 offset:2048\n\t"
      "ds_read_b64_tr_b16 a[246:247], v163 offset:10240\n\t"
      "ds_read_b64_tr_b16 a[184:185], v162 offset:16384\n\t"
      "ds_read_b64_tr_b16 a[186:187], v162 offset:24576\n\t"
      "ds_read_b64_tr_b16 a[188:189], v162 offset:18432\n\t"
      "ds_read_b64_tr_b16 a[190:191], v162 offset:26624\n\t"
      "ds_read_b64_tr_b16 a[248:249], v163 offset:16384\n\t"
      "ds_read_b64_tr_b16 a[250:251], v163 offset:24576\n\t"
      "ds_read_b64_tr_b16 a[252:253], v163 offset:18432\n\t"
      "ds_read_b64_tr_b16 a[254:255], v163 offset:26624\n\t"
      "s_waitcnt vmcnt(24) lgkmcnt(0)\n\t"
      "s_barrier\n\t"
      "s_mov_b64 s[60:61], %[wo]\n\t"
      "s_add_u32 m0, s65, 65536\n\t"
      "s_add_u32 s62, s60, 0\n\t"
      "s_addc_u32 s63, s61, 0\n\t"
      "global_load_lds_dwordx4 %[oe], s[62:63]\n\t"
      "s_add_u32 m0, s65, 66560\n\t"
      "s_add_u32 s62, s60, 2048\n\t"
      "s_addc_u32 s63, s61, 0\n\t"
      "global_load_lds_dwordx4 %[oe], s[62:63]\n\t"
      "s_add_u32 m0, s65, 67584\n\t"
      "s_add_u32 s62, s60, 4096\n\t"
      "s_addc_u32 s63, s61, 0\n\t"
      "global_load_lds_dwordx4 v161, s[62:63]\n\t"
      "s_add_u32 m0, s65, 68608\n\t"
      "s_add_u32 s62, s60, 6144\n\t"
      "s_addc_u32 s63, s61, 0\n\t"
      "global_load_lds_dwordx4 v161, s[62:63]\n\t"
      "s_add_u32 m0, s65, 69632\n\t"
      "s_add_u32 s62, s60, 8192\n\t"
      "s_addc_u32 s63, s61, 0\n\t"
      "global_load_lds_dwordx4 %[oe], s[62:63]\n\t"
      "s_add_u32 m0, s65, 70656\n\t"
      "s_add_u32 s62, s60, 10240\n\t"
      "s_addc_u32 s63, s61, 0\n\t"
      "global_load_lds_dwordx4 %[oe], s[62:63]\n\t"
      "s_add_u32 m0, s65, 71680\n\t"
      "s_add_u32 s62, s60, 12288\n\t"
      "s_addc_u32 s63, s61, 0\n\t"
      "global_load_lds_dwordx4 v161, s[62:63]\n\t"
      "s_add_u32 m0, s65, 72704\n\t"
      "s_add_u32 s62, s60, 14336\n\t"
      "s_addc_u32 s63, s61, 0\n\t"
      "global_load_lds_dwordx4 v161, s[62:63]\n\t"
      "v_add_u32 v162, 98304, %[trq]\n\t"
      "v_xor_b32 v163, 32, v162\n\t"
      "ds_read_b64_tr_b16 v[32:33], v162 offset:0\n\t"
      "ds_read_b64_tr_b16 v[34:35], v162 offset:8192\n\t"
      "ds_read_b64_tr_b16 v[36:37], v162 offset:2048\n\t"
      "ds_read_b64_tr_b16 v[38:39], v162 offset:10240\n\t"
      "ds_read_b64_tr_b16 v[64:65], v163 offset:0\n\t"
      "ds_read_b64_tr_b16 v[66:67], v163 offset:8192\n\t"
      "ds_read_b64_tr_b16 v[68:69], v163 offset:2048\n\t"
      "ds_read_b64_tr_b16 v[70:71], v163 offset:10240\n\t"
      "ds_read_b64_tr_b16 v[40:41], v162 offset:16384\n\t"
      "ds_read_b64_tr_b16 v[42:43], v162 offset:24576\n\t"
      "ds_read_b64_tr_b16 v[44:45], v162 offset:18432\n\t"
      "ds_read_b64_tr_b16 v[46:47], v162 offset:26624\n\t"
      "ds_read_b64_tr_b16 v[72:73], v163 offset:16384\n\t"
      "ds_read_b64_tr_b16 v[74:75], v163 offset:24576\n\t"
      "ds_read_b64_tr_b16 v[76:77], v163 offset:18432\n\t"
      "ds_read_b64_tr_b16 v[78:79], v163 offset:26624\n\t"
      "s_waitcnt vmcnt(24) lgkmcnt(0)\n\t"
      "s_barrier\n\t"
      "s_add_u32 m0, s65, 98304\n\t"
      "s_add_u32 s62, s60, 65536\n\t"
      "s_addc_u32 s63, s61, 0\n\t"
      "global_load_lds_dwordx4 %[oe], s[62:63]\n\t"
      "s_add_u32 m0, s65, 99328\n\t"
      "s_add_u32 s62, s60, 67584\n\t"
      "s_addc_u32 s63, s61, 0\n\t"
      "global_load_lds_dwordx4 %[oe], s[62:63]\n\t"
      "s_add_u32 m0, s65, 100352\n\t"
      "s_add_u32 s62, s60, 69632\n\t"
      "s_addc_u32 s63, s61, 0\n\t"
      "global_load_lds_dwordx4 v161, s[62:63]\n\t"
      "s_add_u32 m0, s65, 101376\n\t"
      "s_add_u32 s62, s60, 71680\n\t"
      "s_addc_u32 s63, s61, 0\n\t"
      "global_load_lds_dwordx4 v161, s[62:63]\n\t"
      "s_add_u32 m0, s65, 102400\n\t"
      "s_add_u32 s62, s60, 73728\n\t"
      "s_addc_u32 s63, s61, 0\n\t"
      "global_load_lds_dwordx4 %[oe], s[62:63]\n\t"
      "s_add_u32 m0, s65, 103424\n\t"
      "s_add_u32 s62, s60, 75776\n\t"
      "s_addc_u32 s63, s61, 0\n\t"
      "global_load_lds_dwordx4 %[oe], s[62:63]\n\t"
      "s_add_u32 m0, s65, 104448\n\t"
      "s_add_u32 s62, s60, 77824\n\t"
      "s_addc_u32 s63, s61, 0\n\t"
      "global_load_lds_dwordx4 v161, s[62:63]\n\t"
      "s_add_u32 m0, s65, 105472\n\t"
      "s_add_u32 s62, s60, 79872\n\t"
      "s_addc_u32 s63, s61, 0\n\t"
      "global_load_lds_dwordx4 v161, s[62:63]\n\t"
      "v_add_u32 v162, 131072, %[trq]\n\t"
      "v_xor_b32 v163, 32, v162\n\t"
      "ds_read_b64_tr_b16 v[48:49], v162 offset:0\n\t"
      "ds_read_b64_tr_b16 v[50:51], v162 offset:8192\n\t"
      "ds_read_b64_tr_b16 v[52:53], v162 offset:2048\n\t"
      "ds_read_b64_tr_b16 v[54:55], v162 offset:10240\n\t"
      "ds_read_b64_tr_b16 v[80:81], v163 offset:0\n\t"
      "ds_read_b64_tr_b16 v[82:83], v163 offset:8192\n\t"
      "ds_read_b64_tr_b16 v[84:85], v163 offset:2048\n\t"
      "ds_read_b64_tr_b16 v[86:87], v163 offset:10240\n\t"
      "ds_read_b64_tr_b16 v[56:57], v162 offset:16384\n\t"
      "ds_read_b64_tr_b16 v[58:59], v162 offset:24576\n\t"
      "ds_read_b64_tr_b16 v[60:61], v162 offset:18432\n\t"
      "ds_read_b64_tr_b16 v[62:63], v162 offset:26624\n\t"
      "ds_read_b64_tr_b16 v[88:89], v163 offset:16384\n\t"
      "ds_read_b64_tr_b16 v[90:91], v163 offset:24576\n\t"
      "ds_read_b64_tr_b16 v[92:93], v163 offset:18432\n\t"
      "ds_read_b64_tr_b16 v[94:95], v163 offset:26624\n\t"
      "s_waitcnt vmcnt(24) lgkmcnt(0)\n\t"
      "s_barrier\n\t"
      "v_add_u32 v162, 0, %[trq]\n\t"
      "v_xor_b32 v163, 32, v162\n\t"
      "ds_read_b64_tr_b16 v[192:193], v162 offset:0\n\t"
      "ds_read_b64_tr_b16 v[194:195], v162 offset:8192\n\t"
      "ds_read_b64_tr_b16 v[196:197], v162 offset:2048\n\t"
      "ds_read_b64_tr_b16 v[198:199], v162 offset:10240\n\t"
      "ds_read_b64_tr_b16 v[200:201], v163 offset:0\n\t"
      "ds_read_b64_tr_b16 v[202:203], v163 offset:8192\n\t"
      "ds_read_b64_tr_b16 v[204:205], v163 offset:2048\n\t"
      "ds_read_b64_tr_b16 v[206:207], v163 offset:10240\n\t"
      "ds_read_b64_tr_b16 v[208:209], v162 offset:16384\n\t"
      "ds_read_b64_tr_b16 v[210:211], v162 offset:24576\n\t"
      "ds_read_b64_tr_b16 v[212:213], v162 offset:18432\n\t"
      "ds_read_b64_tr_b16 v[214:215], v162 offset:26624\n\t"
      "ds_read_b64_tr_b16 v[216:217], v163 offset:16384\n\t"
      "ds_read_b64_tr_b16 v[218:219], v163 offset:24576\n\t"
      "ds_read_b64_tr_b16 v[220:221], v163 offset:18432\n\t"
      "ds_read_b64_tr_b16 v[222:223], v163 offset:26624\n\t"
      "s_waitcnt vmcnt(16) lgkmcnt(0)\n\t"
      "s_barrier\n\t"
      "v_add_u32 v162, 32768, %[trq]\n\t"
      "v_xor_b32 v163, 32, v162\n\t"
      "ds_read_b64_tr_b16 v[224:225], v162 offset:0\n\t"
      "ds_read_b64_tr_b16 v[226:227], v162 offset:8192\n\t"
      "ds_read_b64_tr_b16 v[228:229], v162 offset:2048\n\t"
      "ds_read_b64_tr_b16 v[230:231], v162 offset:10240\n\t"
      "ds_read_b64_tr_b16 v[232:233], v163 offset:0\n\t"
      "ds_read_b64_tr_b16 v[234:235], v163 offset:8192\n\t"
      "ds_read_b64_tr_b16 v[236:237], v163 offset:2048\n\t"
      "ds_read_b64_tr_b16 v[238:239], v163 offset:10240\n\t"
      "ds_read_b64_tr_b16 v[240:241], v162 offset:16384\n\t"
      "ds_read_b64_tr_b16 v[242:243], v162 offset:24576\n\t"
      "ds_read_b64_tr_b16 v[244:245], v162 offset:18432\n\t"
      "ds_read_b64_tr_b16 v[246:247], v162 offset:26624\n\t"
      "ds_read_b64_tr_b16 v[248:249], v163 offset:16384\n\t"
      "ds_read_b64_tr_b16 v[250:251], v163 offset:24576\n\t"
      "ds_read_b64_tr_b16 v[252:253], v163 offset:18432\n\t"
      "ds_read_b64_tr_b16 v[254:255], v163 offset:26624\n\t"
      "s_waitcnt vmcnt(8) lgkmcnt(0)\n\t"
      "s_barrier\n\t"
      "v_add_u32 v162, 65536, %[tro]\n\t"
      "v_xor_b32 v163, 32, v162\n\t"
      "v_xor_b32 v164, 64, v162\n\t"
      "v_xor_b32 v165, 96, v162\n\t"
      "ds_read_b64_tr_b16 v[96:97], v162 offset:0\n\t"
      "ds_read_b64_tr_b16 v[98:99], v162 offset:1024\n\t"
      "ds_read_b64_tr_b16 v[100:101], v162 offset:16384\n\t"
      "ds_read_b64_tr_b16 v[102:103], v162 offset:17408\n\t"
      "ds_read_b64_tr_b16 v[112:113], v163 offset:0\n\t"
      "ds_read_b64_tr_b16 v[114:115], v163 offset:1024\n\t"
      "ds_read_b64_tr_b16 v[116:117], v163 offset:16384\n\t"
      "ds_read_b64_tr_b16 v[118:119], v163 offset:17408\n\t"
      "ds_read_b64_tr_b16 v[128:129], v164 offset:0\n\t"
      "ds_read_b64_tr_b16 v[130:131], v164 offset:1024\n\t"
      "ds_read_b64_tr_b16 v[132:133], v164 offset:16384\n\t"
      "ds_read_b64_tr_b16 v[134:135], v164 offset:17408\n\t"
      "ds_read_b64_tr_b16 v[144:145], v165 offset:0\n\t"
      "ds_read_b64_tr_b16 v[146:147], v165 offset:1024\n\t"
      "ds_read_b64_tr_b16 v[148:149], v165 offset:16384\n\t"
      "ds_read_b64_tr_b16 v[150:151], v165 offset:17408\n\t"
      "s_waitcnt vmcnt(0) lgkmcnt(0)\n\t"
      "s_barrier\n\t"
      "v_add_u32 v162, 98304, %[tro]\n\t"
      "v_xor_b32 v163, 32, v162\n\t"
      "v_xor_b32 v164, 64, v162\n\t"
      "v_xor_b32 v165, 96, v162\n\t"
      "ds_read_b64_tr_b16 v[104:105], v162 offset:0\n\t"
      "ds_read_b64_tr_b16 v[106:107], v162 offset:1024\n\t"
      "ds_read_b64_tr_b16 v[108:109], v162 offset:16384\n\t"
      "ds_read_b64_tr_b16 v[110:111], v162 offset:17408\n\t"
      "ds_read_b64_tr_b16 v[120:121], v163 offset:0\n\t"
      "ds_read_b64_tr_b16 v[122:123], v163 offset:1024\n\t"
      "ds_read_b64_tr_b16 v[124:125], v163 offset:16384\n\t"
      "ds_read_b64_tr_b16 v[126:127], v163 offset:17408\n\t"
      "ds_read_b64_tr_b16 v[136:137], v164 offset:0\n\t"
      "ds_read_b64_tr_b16 v[138:139], v164 offset:1024\n\t"
      "ds_read_b64_tr_b16 v[140:141], v164 offset:16384\n\t"
      "ds_read_b64_tr_b16 v[142:143], v164 offset:17408\n\t"
      "ds_read_b64_tr_b16 v[152:153], v165 offset:0\n\t"
      "ds_read_b64_tr_b16 v[154:155], v165 offset:1024\n\t"
      "ds_read_b64_tr_b16 v[156:157], v165 offset:16384\n\t"
      "ds_read_b64_tr_b16 v[158:159], v165 offset:17408\n\t"
      "s_waitcnt lgkmcnt(0)\n\t"
      "s_barrier\n\t"
      "ds_write_b128 %[wvrd], v[32:35] offset:0\n\t"
      "ds_write_b128 %[wvrd], v[36:39] offset:1024\n\t"
      "ds_write_b128 %[wvrd], v[40:43] offset:2048\n\t"
      "ds_write_b128 %[wvrd], v[44:47] offset:3072\n\t"
      "ds_write_b128 %[wvrd], v[48:51] offset:4096\n\t"
      "ds_write_b128 %[wvrd], v[52:55] offset:5120\n\t"
      "ds_write_b128 %[wvrd], v[56:59] offset:6144\n\t"
      "ds_write_b128 %[wvrd], v[60:63] offset:7168\n\t"
      "ds_write_b128 %[wvrd], v[64:67] offset:8192\n\t"
      "ds_write_b128 %[wvrd], v[68:71] offset:9216\n\t"
      "ds_write_b128 %[wvrd], v[72:75] offset:10240\n\t"
      "ds_write_b128 %[wvrd], v[76:79] offset:11264\n\t"
      "ds_write_b128 %[wvrd], v[80:83] offset:12288\n\t"
      "ds_write_b128 %[wvrd], v[84:87] offset:13312\n\t"
      "ds_write_b128 %[wvrd], v[88:91] offset:14336\n\t"
      "ds_write_b128 %[wvrd], v[92:95] offset:15360\n\t"
      "ds_write_b128 %[wvrd], v[96:99] offset:16384\n\t"
      "ds_write_b128 %[wvrd], v[100:103] offset:17408\n\t"
      "ds_write_b128 %[wvrd], v[104:107] offset:18432\n\t"
      "ds_write_b128 %[wvrd], v[108:111] offset:19456\n\t"
      "ds_write_b128 %[wvrd], v[112:115] offset:20480\n\t"
      "ds_write_b128 %[wvrd], v[116:119] offset:21504\n\t"
      "ds_write_b128 %[wvrd], v[120:123] offset:22528\n\t"
      "ds_write_b128 %[wvrd], v[124:127] offset:23552\n\t"
      "ds_write_b128 %[wvrd], v[128:131] offset:24576\n\t"
      "ds_write_b128 %[wvrd], v[132:135] offset:25600\n\t"
      "ds_write_b128 %[wvrd], v[136:139] offset:26624\n\t"
      "ds_write_b128 %[wvrd], v[140:143] offset:27648\n\t"
      "ds_write_b128 %[wvrd], v[144:147] offset:28672\n\t"
      "ds_write_b128 %[wvrd], v[148:151] offset:29696\n\t"
      "ds_write_b128 %[wvrd], v[152:155] offset:30720\n\t"
      "ds_write_b128 %[wvrd], v[156:159] offset:31744\n\t"
      "s_mov_b32 s68, 0x05040100\n\t"
      "s_mov_b32 s69, 0x07060302\n\t"
      "s_lshl_b32 s52, s94, 10\n\t"
      "v_add_u32 v176, s52, %[memrd]\n\t"
      "s_waitcnt vmcnt(0)\n\t"
      "v_perm_b32 v168, v181, v180, s68\n\t"
      "v_perm_b32 v169, v183, v182, s68\n\t"
      "v_perm_b32 v170, v181, v180, s69\n\t"
      "v_perm_b32 v171, v183, v182, s69\n\t"
      "v_perm_b32 v172, v185, v184, s68\n\t"
      "v_perm_b32 v173, v187, v186, s68\n\t"
      "v_perm_b32 v174, v185, v184, s69\n\t"
      "v_perm_b32 v175, v187, v186, s69\n\t"
      "ds_write_b128 v176, v[168:171]\n\t"
      "ds_write_b128 v176, v[172:175] offset:4096\n\t"
      "s_mov_b32 s98, 71303168\n\t"
      "s_lshl_b32 s52, s94, 11\n\t"
      "s_add_u32 s41, s52, 917504\n\t"
      "s_lshl_b32 s53, s92, 14\n\t"
      "s_add_u32 s44, s53, s52\n\t"
      "s_add_u32 s44, s44, 3014656\n\t"
      "s_lshl_b32 s46, s92, 12\n\t"
      "s_add_u32 s46, s46, 36700160\n\t"
      "s_lshl_b32 s55, s92, 6\n\t"
      "s_add_u32 s55, s55, 1048576\n\t"
      "s_lshl_b32 s51, s92, 7\n\t"
      "s_lshl_b32 s53, s93, 6\n\t"
      "s_add_u32 s51, s51, s53\n\t"
      "s_add_u32 s51, s51, 1050624\n\t"
      "v_add_u32 v160, s52, %[memrd]\n\t"
      "v_mov_b32 v161, 0x80000000\n\t"
      "v_cmp_ne_u32 vcc, 131072, v160\n\t"
      "s_nop 1\n\t"
      "v_cndmask_b32 %[trq], 0, v161, vcc\n\t"
      "v_mov_b32 %[oe], -1\n\t"
      "s_add_u32 s47, s52, 139264\n\t"
      "s_add_u32 s48, s52, 147456\n\t"
      "s_lshl_b32 s66, s94, 10\n\t"
      "s_mov_b32 s67, 0x3e0293ee\n\t"
      "s_mov_b32 s56, 0x1f800000\n\t"
      "s_mov_b32 s57, 0x40000000\n\t"
      "s_mov_b32 s58, 0xcccccccc\n\t"
      "s_mov_b32 s59, 0xcccccccc\n\t"
      "v_add_u32 %[tro], 4096, %[xwr]\n\t"
      "v_add_u32 %[m0o], s52, %[memrd]\n\t"
      "s_mov_b32 s40, 0\n\t"
      "s_lshr_b32 s53, s92, 3\n\t"
      "s_lshl_b32 s50, s53, 4\n\t"
      "s_mov_b32 s31, 1054720\n\t"
      "s_lshl_b32 s30, s53, 1\n\t"
      "s_add_u32 s30, s30, s31\n\t"
      "s_and_b32 s53, %[haw], 0xffff07\n\t"
      "s_cmp_eq_u32 s53, 0\n\t"
      "s_cselect_b32 s65, 0, 1\n\t"
      "s_mov_b32 s27, 0\n\t"
      "s_mov_b32 s53, 2118912\n\t"
      "s_mov_b64 s[32:33], %[tag]\n\t"
      "L_sel_%=:\n\t"
      "buffer_load_dwordx2 v[158:159], off, s[96:99], s53 sc1\n\t"
      "s_waitcnt vmcnt(0)\n\t"
      "v_readfirstlane_b32 s54, v158\n\t"
      "v_readfirstlane_b32 s34, v159\n\t"
      "s_nop 2\n\t"
      "s_sub_u32 s54, s54, s32\n\t"
      "s_xor_b32 s34, s34, s33\n\t"
      "s_lshr_b32 s35, s54, 7\n\t"
      "s_or_b32 s34, s34, s35\n\t"
      "s_cmp_eq_u32 s34, 0\n\t"
      "s_cbranch_scc1 L_sel_done_%=\n\t"
      "s_add_u32 s49, s49, 1\n\t"
      "s_cmp_lt_u32 s49, 100000\n\t"
      "s_cbranch_scc1 L_sel_%=\n\t"
      "L_sel_done_%=:\n\t"
      "s_and_b32 s54, s54, 1\n\t"
      "s_lshl_b32 s42, s54, 13\n\t"
      "s_add_u32 s41, s41, s42\n\t"
      "s_add_u32 s43, s42, 16384\n\t"
      "s_add_u32 s55, s55, s42\n\t"
      "s_add_u32 s51, s51, s42\n\t"
      "s_add_u32 s30, s30, s42\n\t"
      "s_add_u32 s31, s31, s42\n\t"
      "s_cmp_eq_u32 s65, 0\n\t"
      "s_cbranch_scc1 L_clk0_%=\n\t"
      "L_clk0b_%=:\n\t"
      "L_kv0_%=:\n\t"
      "buffer_load_dword v159, off, s[96:99], s55 sc1\n\t"
      "s_waitcnt vmcnt(0)\n\t"
      "v_readfirstlane_b32 s54, v159\n\t"
      "s_nop 3\n\t"
      "s_cmp_ge_i32 s54, 0\n\t"
      "s_cbranch_scc1 L_kv0_done_%=\n\t"
      "s_add_u32 s49, s49, 1\n\t"
      "s_cmp_lt_u32 s49, 100000\n\t"
      "s_cbranch_scc1 L_kv0_%=\n\t"
      "s_mov_b32 s53, 2118656\n\t"
      "v_mov_b32 v159, 1\n\t"
      "buffer_store_dword v159, off, s[96:99], s53 sc1\n\t"
      "L_kv0_done_%=:\n\t"
      "s_add_u32 s45, s44, 8192\n\t"
      "s_mov_b32 m0, s47\n\t"
      "s_nop 0\n\t"
      "buffer_load_dwordx4 %[memrd], s[96:99], s44 offen sc1 lds\n\t"
      "buffer_load_dwordx4 %[memrd], s[96:99], s44 offen offset:1024 sc1 lds\n\t"
      "s_mov_b32 m0, s48\n\t"
      "s_nop 0\n\t"
      "buffer_load_dwordx4 %[memrd], s[96:99], s45 offen sc1 lds\n\t"
      "buffer_load_dwordx4 %[memrd], s[96:99], s45 offen offset:1024 sc1 lds\n\t"
      "s_add_u32 s44, s44, 524288\n\t"
      "s_add_u32 s45, s44, 8192\n\t"
      "s_mov_b64 s[60:61], s[96:97]\n\t"
      "s_mov_b32 s62, 0\n\t"
      "s_mov_b32 s63, s99\n\t"
      "s_waitcnt vmcnt(0) lgkmcnt(0)\n\t"
      "s_barrier\n\t"
      "ds_read_b128 v[160:163], %[wvrd] offset:0\n\t"
      "ds_read_b128 v[164:167], %[wvrd] offset:1024\n\t"
      "ds_read_b128 v[168:171], %[wvrd] offset:8192\n\t"
      "ds_read_b128 v[172:175], %[wvrd] offset:9216\n\t"
      "ds_read_b128 v[176:179], %[wvrd] offset:2048\n\t"
      "ds_read_b128 v[180:183], %[wvrd] offset:3072\n\t"
      "ds_read_b128 v[184:187], %[wvrd] offset:10240\n\t"
      "ds_read_b128 v[188:191], %[wvrd] offset:11264\n\t"
      "v_mov_b64 v[120:121], 0\n\t"
      "v_mov_b64 v[122:123], 0\n\t"
      "v_mov_b64 v[124:125], 0\n\t"
      "v_mov_b64 v[126:127], 0\n\t"
      "v_mov_b64 v[128:129], 0\n\t"
      "v_mov_b64 v[130:131], 0\n\t"
      "v_mov_b64 v[132:133], 0\n\t"
      "v_mov_b64 v[134:135], 0\n\t"
      "v_mov_b64 v[136:137], 0\n\t"
      "v_mov_b64 v[138:139], 0\n\t"
      "v_mov_b64 v[140:141], 0\n\t"
      "v_mov_b64 v[142:143], 0\n\t"
      "s_mov_b64 s[68:69], -1\n\t"
      "s_cmp_lg_u32 s94, 0\n\t"
      "s_cbranch_scc1 L_nw0_%=\n\t"
      "s_getpc_b64 s[38:39]\n\t"
      "L_pc0_%=:\n\t"
      "s_add_u32 s38, s38, L_poll2_0_%=-L_pc0_%=\n\t"
      "s_addc_u32 s39, s39, 0\n\t"
      "ds_read_b128 v[32:35], %[memrd] offset:0\n\t"
      "ds_read_b128 v[36:39], %[memrd] offset:1024\n\t"
      "s_mov_b64 vcc, 0\n\t"
      "s_waitcnt lgkmcnt(0)\n\t"
      "s_branch L_go0_%=\n\t"
      "L_nw0_%=:\n\t"
      "s_cmp_lg_u32 s94, 1\n\t"
      "s_cbranch_scc1 L_nw1_%=\n\t"
      "s_getpc_b64 s[38:39]\n\t"
      "L_pc1_%=:\n\t"
      "s_add_u32 s38, s38, L_poll2_1_%=-L_pc1_%=\n\t"
      "s_addc_u32 s39, s39, 0\n\t"
      "ds_read_b128 v[40:43], %[memrd] offset:2048\n\t"
      "ds_read_b128 v[44:47], %[memrd] offset:3072\n\t"
      "s_mov_b64 vcc, 0\n\t"
      "s_waitcnt lgkmcnt(0)\n\t"
      "s_branch L_go1_%=\n\t"
      "L_nw1_%=:\n\t"
      "s_cmp_lg_u32 s94, 2\n\t"
      "s_cbranch_scc1 L_nw2_%=\n\t"
      "s_getpc_b64 s[38:39]\n\t"
      "L_pc2_%=:\n\t"
      "s_add_u32 s38, s38, L_poll2_2_%=-L_pc2_%=\n\t"
      "s_addc_u32 s39, s39, 0\n\t"
      "ds_read_b128 v[48:51], %[memrd] offset:4096\n\t"
      "ds_read_b128 v[52:55], %[memrd] offset:5120\n\t"
      "s_mov_b64 vcc, 0\n\t"
      "s_waitcnt lgkmcnt(0)\n\t"
      "s_branch L_go2_%=\n\t"
      "L_nw2_%=:\n\t"
      "s_cmp_lg_u32 s94, 3\n\t"
      "s_cbranch_scc1 L_nw3_%=\n\t"
      "s_getpc_b64 s[38:39]\n\t"
      "L_pc3_%=:\n\t"
      "s_add_u32 s38, s38, L_poll2_3_%=-L_pc3_%=\n\t"
      "s_addc_u32 s39, s39, 0\n\t"
      "ds_read_b128 v[56:59], %[memrd] offset:6144\n\t"
      "ds_read_b128 v[60:63], %[memrd] offset:7168\n\t"
      "s_mov_b64 vcc, 0\n\t"
      "s_waitcnt lgkmcnt(0)\n\t"
      "s_branch L_go3_%=\n\t"
      "L_nw3_%=:\n\t"
      "L_poll2_0_%=:\n\t"
      "buffer_load_dwordx4 v[32:35], %[memrd], s[96:99], s41 offen sc1\n\t"
      "buffer_load_dwordx4 v[36:39], %[memrd], s[96:99], s41 offen offset:1024 sc1\n\t"
      "s_waitcnt vmcnt(1)\n\t"
      "ds_write_b128 %[mwr], v[32:35]\n\t"
      "v_max3_u32 v159, v32, v33, v34\n\t"
      "s_waitcnt vmcnt(0)\n\t"
      "ds_write_b128 %[mwr], v[36:39] offset:1024\n\t"
      "v_max3_u32 v159, v159, v35, v36\n\t"
      "v_max3_u32 v159, v159, v37, v38\n\t"
      "v_max_u32 v159, v159, v39\n\t"
      "v_cmp_eq_u32 vcc, -1, v159\n\t"
      "L_go0_%=:\n\t"
      "v_smfmac_f32_16x16x64_bf16 v[120:123], v[32:35], a[0:7], %[idx]\n\t"
      "s_and_b64 vcc, vcc, s[68:69]\n\t"
      "v_smfmac_f32_16x16x64_bf16 v[124:127], v[32:35], a[64:71], %[idx]\n\t"
      "s_cbranch_vccnz L_retry2_%=\n\t"
      "v_smfmac_f32_16x16x64_bf16 v[120:123], v[36:39], a[8:15], %[idx]\n\t"
      "v_smfmac_f32_16x16x64_bf16 v[124:127], v[36:39], a[72:79], %[idx]\n\t"
      "v_smfmac_f32_16x16x64_bf16 v[128:131], v[32:35], a[128:135], %[idx]\n\t"
      "s_waitcnt lgkmcnt(0)\n\t"
      "s_barrier\n\t"
      "ds_read_b128 v[40:43], %[memrd] offset:2048\n\t"
      "ds_read_b128 v[44:47], %[memrd] offset:3072\n\t"
      "v_smfmac_f32_16x16x64_bf16 v[132:135], v[32:35], a[192:199], %[idx]\n\t"
      "ds_read_b128 v[48:51], %[memrd] offset:4096\n\t"
      "v_smfmac_f32_16x16x64_bf16 v[128:131], v[36:39], a[136:143], %[idx]\n\t"
      "buffer_store_dword %[oe], %[trq], s[96:99], s51 offen sc1\n\t"
      "s_cmp_lg_u32 s27, 0\n\t"
      "v_smfmac_f32_16x16x64_bf16 v[132:135], v[36:39], a[200:207], %[idx]\n\t"
      "s_cbranch_scc1 L_cst0_%=\n\t"
      "L_cstb0_%=:\n\t"
      "ds_read_b128 v[52:55], %[memrd] offset:5120\n\t"
      "s_waitcnt lgkmcnt(3)\n\t"
      "v_smfmac_f32_16x16x64_bf16 v[120:123], v[40:43], a[16:23], %[idx]\n\t"
      "ds_read_b128 v[56:59], %[memrd] offset:6144\n\t"
      "v_smfmac_f32_16x16x64_bf16 v[124:127], v[40:43], a[80:87], %[idx]\n\t"
      "ds_read_b128 v[60:63], %[memrd] offset:7168\n\t"
      "s_waitcnt lgkmcnt(4)\n\t"
      "v_smfmac_f32_16x16x64_bf16 v[120:123], v[44:47], a[24:31], %[idx]\n\t"
      "ds_read_b128 v[80:83], %[memrd] offset:12288\n\t"
      "v_smfmac_f32_16x16x64_bf16 v[124:127], v[44:47], a[88:95], %[idx]\n\t"
      "ds_read_b128 v[84:87], %[memrd] offset:13312\n\t"
      "s_waitcnt lgkmcnt(5)\n\t"
      "v_smfmac_f32_16x16x64_bf16 v[120:123], v[48:51], a[32:39], %[idx]\n\t"
      "ds_read_b128 v[88:91], %[memrd] offset:14336\n\t"
      "v_smfmac_f32_16x16x64_bf16 v[124:127], v[48:51], a[96:103], %[idx]\n\t"
      "ds_read_b128 v[92:95], %[memrd] offset:15360\n\t"
      "s_waitcnt lgkmcnt(6)\n\t"
      "v_smfmac_f32_16x16x64_bf16 v[120:123], v[52:55], a[40:47], %[idx]\n\t"
      "ds_read_b128 v[64:67], %[memrd] offset:8192\n\t"
      "v_smfmac_f32_16x16x64_bf16 v[124:127], v[52:55], a[104:111], %[idx]\n\t"
      "ds_read_b128 v[68:71], %[memrd] offset:9216\n\t"
      "s_waitcnt lgkmcnt(7)\n\t"
      "v_smfmac_f32_16x16x64_bf16 v[120:123], v[56:59], a[48:55], %[idx]\n\t"
      "ds_read_b128 v[72:75], %[memrd] offset:10240\n\t"
      "v_smfmac_f32_16x16x64_bf16 v[124:127], v[56:59], a[112:119], %[idx]\n\t"
      "ds_read_b128 v[76:79], %[memrd] offset:11264\n\t"
      "s_waitcnt lgkmcnt(8)\n\t"
      "v_smfmac_f32_16x16x64_bf16 v[120:123], v[60:63], a[56:63], %[idx]\n\t"
      "ds_read_b128 v[96:99], %[m0o] offset:16384\n\t"
      "v_smfmac_f32_16x16x64_bf16 v[124:127], v[60:63], a[120:127], %[idx]\n\t"
      "ds_read_b128 v[100:103], %[m0o] offset:17408\n\t"
      "v_smfmac_f32_16x16x64_bf16 v[128:131], v[40:43], a[144:151], %[idx]\n\t"
      "buffer_load_dword %[qkv], off, s[96:99], s55 sc1\n\t"
      "s_nop 1\n\t"
      "v_smfmac_f32_16x16x64_bf16 v[132:135], v[40:43], a[208:215], %[idx]\n\t"
      "buffer_load_dwordx2 v[158:159], off, s[96:99], s31 sc1\n\t"
      "s_nop 1\n\t"
      "v_smfmac_f32_16x16x64_bf16 v[128:131], v[44:47], a[152:159], %[idx]\n\t"
      "v_add_f32 v120, v120, v122\n\t"
      "v_add_f32 v121, v121, v123\n\t"
      "v_smfmac_f32_16x16x64_bf16 v[132:135], v[44:47], a[216:223], %[idx]\n\t"
      "v_add_f32 v124, v124, v126\n\t"
      "v_smfmac_f32_16x16x64_bf16 v[128:131], v[48:51], a[160:167], %[idx]\n\t"
      "v_add_f32 v125, v125, v127\n\t"
      "v_smfmac_f32_16x16x64_bf16 v[132:135], v[48:51], a[224:231], %[idx]\n\t"
      "v_mul_f32 v120, s67, v120\n\t"
      "v_mul_f32 v121, s67, v121\n\t"
      "v_smfmac_f32_16x16x64_bf16 v[128:131], v[52:55], a[168:175], %[idx]\n\t"
      "v_mul_f32 v124, s67, v124\n\t"
      "v_smfmac_f32_16x16x64_bf16 v[132:135], v[52:55], a[232:239], %[idx]\n\t"
      "v_mul_f32 v125, s67, v125\n\t"
      "v_smfmac_f32_16x16x64_bf16 v[128:131], v[56:59], a[176:183], %[idx]\n\t"
      "v_cvt_pk_bf16_f32 v152, v120, v121\n\t"
      "v_cvt_pk_bf16_f32 v153, v124, v125\n\t"
      "v_smfmac_f32_16x16x64_bf16 v[132:135], v[56:59], a[240:247], %[idx]\n\t"
      "ds_write2_b32 %[xwr], v152, v152 offset0:0 offset1:1\n\t"
      "v_smfmac_f32_16x16x64_bf16 v[128:131], v[60:63], a[184:191], %[idx]\n\t"
      "ds_write2_b32 %[xwr], v153, v153 offset0:128 offset1:129\n\t"
      "v_smfmac_f32_16x16x64_bf16 v[132:135], v[60:63], a[248:255], %[idx]\n\t"
      "s_branch L_join_%=\n\t"
      "L_poll2_1_%=:\n\t"
      "buffer_load_dwordx4 v[40:43], %[memrd], s[96:99], s41 offen sc1\n\t"
      "buffer_load_dwordx4 v[44:47], %[memrd], s[96:99], s41 offen offset:1024 sc1\n\t"
      "s_waitcnt vmcnt(1)\n\t"
      "ds_write_b128 %[mwr], v[40:43]\n\t"
      "v_max3_u32 v159, v40, v41, v42\n\t"
      "s_waitcnt vmcnt(0)\n\t"
      "ds_write_b128 %[mwr], v[44:47] offset:1024\n\t"
      "v_max3_u32 v159, v159, v43, v44\n\t"
      "v_max3_u32 v159, v159, v45, v46\n\t"
      "v_max_u32 v159, v159, v47\n\t"
      "v_cmp_eq_u32 vcc, -1, v159\n\t"
      "L_go1_%=:\n\t"
      "v_smfmac_f32_16x16x64_bf16 v[120:123], v[40:43], a[16:23], %[idx]\n\t"
      "s_and_b64 vcc, vcc, s[68:69]\n\t"
      "v_smfmac_f32_16x16x64_bf16 v[124:127], v[40:43], a[80:87], %[idx]\n\t"
      "s_cbranch_vccnz L_retry2_%=\n\t"
      "v_smfmac_f32_16x16x64_bf16 v[120:123], v[44:47], a[24:31], %[idx]\n\t"
      "v_smfmac_f32_16x16x64_bf16 v[124:127], v[44:47], a[88:95], %[idx]\n\t"
      "v_smfmac_f32_16x16x64_bf16 v[128:131], v[40:43], a[144:151], %[idx]\n\t"
      "s_waitcnt lgkmcnt(0)\n\t"
      "s_barrier\n\t"
      "ds_read_b128 v[48:51], %[memrd] offset:4096\n\t"
      "ds_read_b128 v[52:55], %[memrd] offset:5120\n\t"
      "v_smfmac_f32_16x16x64_bf16 v[132:135], v[40:43], a[208:215], %[idx]\n\t"
      "ds_read_b128 v[56:59], %[memrd] offset:6144\n\t"
      "v_smfmac_f32_16x16x64_bf16 v[128:131], v[44:47], a[152:159], %[idx]\n\t"
      "buffer_store_dword %[oe], %[trq], s[96:99], s51 offen sc1\n\t"
      "s_cmp_lg_u32 s27, 0\n\t"
      "v_smfmac_f32_16x16x64_bf16 v[132:135], v[44:47], a[216:223], %[idx]\n\t"
      "s_cbranch_scc1 L_cst1_%=\n\t"
      "L_cstb1_%=:\n\t"
      "ds_read_b128 v[60:63], %[memrd] offset:7168\n\t"
      "s_waitcnt lgkmcnt(3)\n\t"
      "v_smfmac_f32_16x16x64_bf16 v[120:123], v[48:51], a[32:39], %[idx]\n\t"
      "ds_read_b128 v[32:35], %[memrd] offset:0\n\t"
      "v_smfmac_f32_16x16x64_bf16 v[124:127], v[48:51], a[96:103], %[idx]\n\t"
      "ds_read_b128 v[36:39], %[memrd] offset:1024\n\t"
      "s_waitcnt lgkmcnt(4)\n\t"
      "v_smfmac_f32_16x16x64_bf16 v[120:123], v[52:55], a[40:47], %[idx]\n\t"
      "ds_read_b128 v[80:83], %[memrd] offset:12288\n\t"
      "v_smfmac_f32_16x16x64_bf16 v[124:127], v[52:55], a[104:111], %[idx]\n\t"
      "ds_read_b128 v[84:87], %[memrd] offset:13312\n\t"
      "s_waitcnt lgkmcnt(5)\n\t"
      "v_smfmac_f32_16x16x64_bf16 v[120:123], v[56:59], a[48:55], %[idx]\n\t"
      "ds_read_b128 v[88:91], %[memrd] offset:14336\n\t"
      "v_smfmac_f32_16x16x64_bf16 v[124:127], v[56:59], a[112:119], %[idx]\n\t"
      "ds_read_b128 v[92:95], %[memrd] offset:15360\n\t"
      "s_waitcnt lgkmcnt(6)\n\t"
      "v_smfmac_f32_16x16x64_bf16 v[120:123], v[60:63], a[56:63], %[idx]\n\t"
      "ds_read_b128 v[64:67], %[memrd] offset:8192\n\t"
      "v_smfmac_f32_16x16x64_bf16 v[124:127], v[60:63], a[120:127], %[idx]\n\t"
      "ds_read_b128 v[68:71], %[memrd] offset:9216\n\t"
      "s_waitcnt lgkmcnt(7)\n\t"
      "v_smfmac_f32_16x16x64_bf16 v[120:123], v[32:35], a[0:7], %[idx]\n\t"
      "ds_read_b128 v[72:75], %[memrd] offset:10240\n\t"
      "v_smfmac_f32_16x16x64_bf16 v[124:127], v[32:35], a[64:71], %[idx]\n\t"
      "ds_read_b128 v[76:79], %[memrd] offset:11264\n\t"
      "s_waitcnt lgkmcnt(8)\n\t"
      "v_smfmac_f32_16x16x64_bf16 v[120:123], v[36:39], a[8:15], %[idx]\n\t"
      "ds_read_b128 v[96:99], %[m0o] offset:16384\n\t"
      "v_smfmac_f32_16x16x64_bf16 v[124:127], v[36:39], a[72:79], %[idx]\n\t"
      "ds_read_b128 v[100:103], %[m0o] offset:17408\n\t"
      "v_smfmac_f32_16x16x64_bf16 v[128:131], v[48:51], a[160:167], %[idx]\n\t"
      "buffer_load_dword %[qkv], off, s[96:99], s55 sc1\n\t"
      "s_nop 1\n\t"
      "v_smfmac_f32_16x16x64_bf16 v[132:135], v[48:51], a[224:231], %[idx]\n\t"
      "buffer_load_dwordx2 v[158:159], off, s[96:99], s31 sc1\n\t"
      "s_nop 1\n\t"
      "v_smfmac_f32_16x16x64_bf16 v[128:131], v[52:55], a[168:175], %[idx]\n\t"
      "v_add_f32 v120, v120, v122\n\t"
      "v_add_f32 v121, v121, v123\n\t"
      "v_smfmac_f32_16x16x64_bf16 v[132:135], v[52:55], a[232:239], %[idx]\n\t"
      "v_add_f32 v124, v124, v126\n\t"
      "v_smfmac_f32_16x16x64_bf16 v[128:131], v[56:59], a[176:183], %[idx]\n\t"
      "v_add_f32 v125, v125, v127\n\t"
      "v_smfmac_f32_16x16x64_bf16 v[132:135], v[56:59], a[240:247], %[idx]\n\t"
      "v_mul_f32 v120, s67, v120\n\t"
      "v_mul_f32 v121, s67, v121\n\t"
      "v_smfmac_f32_16x16x64_bf16 v[128:131], v[60:63], a[184:191], %[idx]\n\t"
      "v_mul_f32 v124, s67, v124\n\t"
      "v_smfmac_f32_16x16x64_bf16 v[132:135], v[60:63], a[248:255], %[idx]\n\t"
      "v_mul_f32 v125, s67, v125\n\t"
      "v_smfmac_f32_16x16x64_bf16 v[128:131], v[32:35], a[128:135], %[idx]\n\t"
      "v_cvt_pk_bf16_f32 v152, v120, v121\n\t"
      "v_cvt_pk_bf16_f32 v153, v124, v125\n\t"
      "v_smfmac_f32_16x16x64_bf16 v[132:135], v[32:35], a[192:199], %[idx]\n\t"
      "ds_write2_b32 %[xwr], v152, v152 offset0:0 offset1:1\n\t"
      "v_smfmac_f32_16x16x64_bf16 v[128:131], v[36:39], a[136:143], %[idx]\n\t"
      "ds_write2_b32 %[xwr], v153, v153 offset0:128 offset1:129\n\t"
      "v_smfmac_f32_16x16x64_bf16 v[132:135], v[36:39], a[200:207], %[idx]\n\t"
      "s_branch L_join_%=\n\t"
      "L_poll2_2_%=:\n\t"
      "buffer_load_dwordx4 v[48:51], %[memrd], s[96:99], s41 offen sc1\n\t"
      "buffer_load_dwordx4 v[52:55], %[memrd], s[96:99], s41 offen offset:1024 sc1\n\t"
      "s_waitcnt vmcnt(1)\n\t"
      "ds_write_b128 %[mwr], v[48:51]\n\t"
      "v_max3_u32 v159, v48, v49, v50\n\t"
      "s_waitcnt vmcnt(0)\n\t"
      "ds_write_b128 %[mwr], v[52:55] offset:1024\n\t"
      "v_max3_u32 v159, v159, v51, v52\n\t"
      "v_max3_u32 v159, v159, v53, v54\n\t"
      "v_max_u32 v159, v159, v55\n\t"
      "v_cmp_eq_u32 vcc, -1, v159\n\t"
      "L_go2_%=:\n\t"
      "v_smfmac_f32_16x16x64_bf16 v[120:123], v[48:51], a[32:39], %[idx]\n\t"
      "s_and_b64 vcc, vcc, s[68:69]\n\t"
      "v_smfmac_f32_16x16x64_bf16 v[124:127], v[48:51], a[96:103], %[idx]\n\t"
      "s_cbranch_vccnz L_retry2_%=\n\t"
      "v_smfmac_f32_16x16x64_bf16 v[120:123], v[52:55], a[40:47], %[idx]\n\t"
      "v_smfmac_f32_16x16x64_bf16 v[124:127], v[52:55], a[104:111], %[idx]\n\t"
      "v_smfmac_f32_16x16x64_bf16 v[128:131], v[48:51], a[160:167], %[idx]\n\t"
      "s_waitcnt lgkmcnt(0)\n\t"
      "s_barrier\n\t"
      "ds_read_b128 v[56:59], %[memrd] offset:6144\n\t"
      "ds_read_b128 v[60:63], %[memrd] offset:7168\n\t"
      "v_smfmac_f32_16x16x64_bf16 v[132:135], v[48:51], a[224:231], %[idx]\n\t"
      "ds_read_b128 v[32:35], %[memrd] offset:0\n\t"
      "v_smfmac_f32_16x16x64_bf16 v[128:131], v[52:55], a[168:175], %[idx]\n\t"
      "buffer_store_dword %[oe], %[trq], s[96:99], s51 offen sc1\n\t"
      "s_cmp_lg_u32 s27, 0\n\t"
      "v_smfmac_f32_16x16x64_bf16 v[132:135], v[52:55], a[232:239], %[idx]\n\t"
      "s_cbranch_scc1 L_cst2_%=\n\t"
      "L_cstb2_%=:\n\t"
      "ds_read_b128 v[36:39], %[memrd] offset:1024\n\t"
      "s_waitcnt lgkmcnt(3)\n\t"
      "v_smfmac_f32_16x16x64_bf16 v[120:123], v[56:59], a[48:55], %[idx]\n\t"
      "ds_read_b128 v[40:43], %[memrd] offset:2048\n\t"
      "v_smfmac_f32_16x16x64_bf16 v[124:127], v[56:59], a[112:119], %[idx]\n\t"
      "ds_read_b128 v[44:47], %[memrd] offset:3072\n\t"
      "s_waitcnt lgkmcnt(4)\n\t"
      "v_smfmac_f32_16x16x64_bf16 v[120:123], v[60:63], a[56:63], %[idx]\n\t"
      "ds_read_b128 v[80:83], %[memrd] offset:12288\n\t"
      "v_smfmac_f32_16x16x64_bf16 v[124:127], v[60:63], a[120:127], %[idx]\n\t"
      "ds_read_b128 v[84:87], %[memrd] offset:13312\n\t"
      "s_waitcnt lgkmcnt(5)\n\t"
      "v_smfmac_f32_16x16x64_bf16 v[120:123], v[32:35], a[0:7], %[idx]\n\t"
      "ds_read_b128 v[88:91], %[memrd] offset:14336\n\t"
      "v_smfmac_f32_16x16x64_bf16 v[124:127], v[32:35], a[64:71], %[idx]\n\t"
      "ds_read_b128 v[92:95], %[memrd] offset:15360\n\t"
      "s_waitcnt lgkmcnt(6)\n\t"
      "v_smfmac_f32_16x16x64_bf16 v[120:123], v[36:39], a[8:15], %[idx]\n\t"
      "ds_read_b128 v[64:67], %[memrd] offset:8192\n\t"
      "v_smfmac_f32_16x16x64_bf16 v[124:127], v[36:39], a[72:79], %[idx]\n\t"
      "ds_read_b128 v[68:71], %[memrd] offset:9216\n\t"
      "s_waitcnt lgkmcnt(7)\n\t"
      "v_smfmac_f32_16x16x64_bf16 v[120:123], v[40:43], a[16:23], %[idx]\n\t"
      "ds_read_b128 v[72:75], %[memrd] offset:10240\n\t"
      "v_smfmac_f32_16x16x64_bf16 v[124:127], v[40:43], a[80:87], %[idx]\n\t"
      "ds_read_b128 v[76:79], %[memrd] offset:11264\n\t"
      "s_waitcnt lgkmcnt(8)\n\t"
      "v_smfmac_f32_16x16x64_bf16 v[120:123], v[44:47], a[24:31], %[idx]\n\t"
      "ds_read_b128 v[96:99], %[m0o] offset:16384\n\t"
      "v_smfmac_f32_16x16x64_bf16 v[124:127], v[44:47], a[88:95], %[idx]\n\t"
      "ds_read_b128 v[100:103], %[m0o] offset:17408\n\t"
      "v_smfmac_f32_16x16x64_bf16 v[128:131], v[56:59], a[176:183], %[idx]\n\t"
      "buffer_load_dword %[qkv], off, s[96:99], s55 sc1\n\t"
      "s_nop 1\n\t"
      "v_smfmac_f32_16x16x64_bf16 v[132:135], v[56:59], a[240:247], %[idx]\n\t"
      "buffer_load_dwordx2 v[158:159], off, s[96:99], s31 sc1\n\t"
      "s_nop 1\n\t"
      "v_smfmac_f32_16x16x64_bf16 v[128:131], v[60:63], a[184:191], %[idx]\n\t"
      "v_add_f32 v120, v120, v122\n\t"
      "v_add_f32 v121, v121, v123\n\t"
      "v_smfmac_f32_16x16x64_bf16 v[132:135], v[60:63], a[248:255], %[idx]\n\t"
      "v_add_f32 v124, v124, v126\n\t"
      "v_smfmac_f32_16x16x64_bf16 v[128:131], v[32:35], a[128:135], %[idx]\n\t"
      "v_add_f32 v125, v125, v127\n\t"
      "v_smfmac_f32_16x16x64_bf16 v[132:135], v[32:35], a[192:199], %[idx]\n\t"
      "v_mul_f32 v120, s67, v120\n\t"
      "v_mul_f32 v121, s67, v121\n\t"
      "v_smfmac_f32_16x16x64_bf16 v[128:131], v[36:39], a[136:143], %[idx]\n\t"
      "v_mul_f32 v124, s67, v124\n\t"
      "v_smfmac_f32_16x16x64_bf16 v[132:135], v[36:39], a[200:207], %[idx]\n\t"
      "v_mul_f32 v125, s67, v125\n\t"
      "v_smfmac_f32_16x16x64_bf16 v[128:131], v[40:43], a[144:151], %[idx]\n\t"
      "v_cvt_pk_bf16_f32 v152, v120, v121\n\t"
      "v_cvt_pk_bf16_f32 v153, v124, v125\n\t"
      "v_smfmac_f32_16x16x64_bf16 v[132:135], v[40:43], a[208:215], %[idx]\n\t"
      "ds_write2_b32 %[xwr], v152, v152 offset0:0 offset1:1\n\t"
      "v_smfmac_f32_16x16x64_bf16 v[128:131], v[44:47], a[152:159], %[idx]\n\t"
      "ds_write2_b32 %[xwr], v153, v153 offset0:128 offset1:129\n\t"
      "v_smfmac_f32_16x16x64_bf16 v[132:135], v[44:47], a[216:223], %[idx]\n\t"
      "s_branch L_join_%=\n\t"
      "L_poll2_3_%=:\n\t"
      "buffer_load_dwordx4 v[56:59], %[memrd], s[96:99], s41 offen sc1\n\t"
      "buffer_load_dwordx4 v[60:63], %[memrd], s[96:99], s41 offen offset:1024 sc1\n\t"
      "s_waitcnt vmcnt(1)\n\t"
      "ds_write_b128 %[mwr], v[56:59]\n\t"
      "v_max3_u32 v159, v56, v57, v58\n\t"
      "s_waitcnt vmcnt(0)\n\t"
      "ds_write_b128 %[mwr], v[60:63] offset:1024\n\t"
      "v_max3_u32 v159, v159, v59, v60\n\t"
      "v_max3_u32 v159, v159, v61, v62\n\t"
      "v_max_u32 v159, v159, v63\n\t"
      "v_cmp_eq_u32 vcc, -1, v159\n\t"
      "L_go3_%=:\n\t"
      "v_smfmac_f32_16x16x64_bf16 v[120:123], v[56:59], a[48:55], %[idx]\n\t"
      "s_and_b64 vcc, vcc, s[68:69]\n\t"
      "v_smfmac_f32_16x16x64_bf16 v[124:127], v[56:59], a[112:119], %[idx]\n\t"
      "s_cbranch_vccnz L_retry2_%=\n\t"
      "v_smfmac_f32_16x16x64_bf16 v[120:123], v[60:63], a[56:63], %[idx]\n\t"
      "v_smfmac_f32_16x16x64_bf16 v[124:127], v[60:63], a[120:127], %[idx]\n\t"
      "v_smfmac_f32_16x16x64_bf16 v[128:131], v[56:59], a[176:183], %[idx]\n\t"
      "s_waitcnt lgkmcnt(0)\n\t"
      "s_barrier\n\t"
      "ds_read_b128 v[32:35], %[memrd] offset:0\n\t"
      "ds_read_b128 v[36:39], %[memrd] offset:1024\n\t"
      "v_smfmac_f32_16x16x64_bf16 v[132:135], v[56:59], a[240:247], %[idx]\n\t"
      "ds_read_b128 v[40:43], %[memrd] offset:2048\n\t"
      "v_smfmac_f32_16x16x64_bf16 v[128:131], v[60:63], a[184:191], %[idx]\n\t"
      "buffer_store_dword %[oe], %[trq], s[96:99], s51 offen sc1\n\t"
      "s_cmp_lg_u32 s27, 0\n\t"
      "v_smfmac_f32_16x16x64_bf16 v[132:135], v[60:63], a[248:255], %[idx]\n\t"
      "s_cbranch_scc1 L_cst3_%=\n\t"
      "L_cstb3_%=:\n\t"
      "ds_read_b128 v[44:47], %[memrd] offset:3072\n\t"
      "s_waitcnt lgkmcnt(3)\n\t"
      "v_smfmac_f32_16x16x64_bf16 v[120:123], v[32:35], a[0:7], %[idx]\n\t"
      "ds_read_b128 v[48:51], %[memrd] offset:4096\n\t"
      "v_smfmac_f32_16x16x64_bf16 v[124:127], v[32:35], a[64:71], %[idx]\n\t"
      "ds_read_b128 v[52:55], %[memrd] offset:5120\n\t"
      "s_waitcnt lgkmcnt(4)\n\t"
      "v_smfmac_f32_16x16x64_bf16 v[120:123], v[36:39], a[8:15], %[idx]\n\t"
      "ds_read_b128 v[80:83], %[memrd] offset:12288\n\t"
      "v_smfmac_f32_16x16x64_bf16 v[124:127], v[36:39], a[72:79], %[idx]\n\t"
      "ds_read_b128 v[84:87], %[memrd] offset:13312\n\t"
      "s_waitcnt lgkmcnt(5)\n\t"
      "v_smfmac_f32_16x16x64_bf16 v[120:123], v[40:43], a[16:23], %[idx]\n\t"
      "ds_read_b128 v[88:91], %[memrd] offset:14336\n\t"
      "v_smfmac_f32_16x16x64_bf16 v[124:127], v[40:43], a[80:87], %[idx]\n\t"
      "ds_read_b128 v[92:95], %[memrd] offset:15360\n\t"
      "s_waitcnt lgkmcnt(6)\n\t"
      "v_smfmac_f32_16x16x64_bf16 v[120:123], v[44:47], a[24:31], %[idx]\n\t"
      "ds_read_b128 v[64:67], %[memrd] offset:8192\n\t"
      "v_smfmac_f32_16x16x64_bf16 v[124:127], v[44:47], a[88:95], %[idx]\n\t"
      "ds_read_b128 v[68:71], %[memrd] offset:9216\n\t"
      "s_waitcnt lgkmcnt(7)\n\t"
      "v_smfmac_f32_16x16x64_bf16 v[120:123], v[48:51], a[32:39], %[idx]\n\t"
      "ds_read_b128 v[72:75], %[memrd] offset:10240\n\t"
      "v_smfmac_f32_16x16x64_bf16 v[124:127], v[48:51], a[96:103], %[idx]\n\t"
      "ds_read_b128 v[76:79], %[memrd] offset:11264\n\t"
      "s_waitcnt lgkmcnt(8)\n\t"
      "v_smfmac_f32_16x16x64_bf16 v[120:123], v[52:55], a[40:47], %[idx]\n\t"
      "ds_read_b128 v[96:99], %[m0o] offset:16384\n\t"
      "v_smfmac_f32_16x16x64_bf16 v[124:127], v[52:55], a[104:111], %[idx]\n\t"
      "ds_read_b128 v[100:103], %[m0o] offset:17408\n\t"
      "v_smfmac_f32_16x16x64_bf16 v[128:131], v[32:35], a[128:135], %[idx]\n\t"
      "buffer_load_dword %[qkv], off, s[96:99], s55 sc1\n\t"
      "s_nop 1\n\t"
      "v_smfmac_f32_16x16x64_bf16 v[132:135], v[32:35], a[192:199], %[idx]\n\t"
      "buffer_load_dwordx2 v[158:159], off, s[96:99], s31 sc1\n\t"
      "s_nop 1\n\t"
      "v_smfmac_f32_16x16x64_bf16 v[128:131], v[36:39], a[136:143], %[idx]\n\t"
      "v_add_f32 v120, v120, v122\n\t"
      "v_add_f32 v121, v121, v123\n\t"
      "v_smfmac_f32_16x16x64_bf16 v[132:135], v[36:39], a[200:207], %[idx]\n\t"
      "v_add_f32 v124, v124, v126\n\t"
      "v_smfmac_f32_16x16x64_bf16 v[128:131], v[40:43], a[144:151], %[idx]\n\t"
      "v_add_f32 v125, v125, v127\n\t"
      "v_smfmac_f32_16x16x64_bf16 v[132:135], v[40:43], a[208:215], %[idx]\n\t"
      "v_mul_f32 v120, s67, v120\n\t"
      "v_mul_f32 v121, s67, v121\n\t"
      "v_smfmac_f32_16x16x64_bf16 v[128:131], v[44:47], a[152:159], %[idx]\n\t"
      "v_mul_f32 v124, s67, v124\n\t"
      "v_smfmac_f32_16x16x64_bf16 v[132:135], v[44:47], a[216:223], %[idx]\n\t"
      "v_mul_f32 v125, s67, v125\n\t"
      "v_smfmac_f32_16x16x64_bf16 v[128:131], v[48:51], a[160:167], %[idx]\n\t"
      "v_cvt_pk_bf16_f32 v152, v120, v121\n\t"
      "v_cvt_pk_bf16_f32 v153, v124, v125\n\t"
      "v_smfmac_f32_16x16x64_bf16 v[132:135], v[48:51], a[224:231], %[idx]\n\t"
      "ds_write2_b32 %[xwr], v152, v152 offset0:0 offset1:1\n\t"
      "v_smfmac_f32_16x16x64_bf16 v[128:131], v[52:55], a[168:175], %[idx]\n\t"
      "ds_write2_b32 %[xwr], v153, v153 offset0:128 offset1:129\n\t"
      "v_smfmac_f32_16x16x64_bf16 v[132:135], v[52:55], a[232:239], %[idx]\n\t"
      "L_join_%=:\n\t"
      "v_smfmac_f32_16x16x64_bf16 v[136:139], v[32:35], v[160:167], %[idx]\n\t"
      "s_nop 2\n\t"
      "v_smfmac_f32_16x16x64_bf16 v[140:143], v[32:35], v[168:175], %[idx]\n\t"
      "s_nop 2\n\t"
      "v_add_f32 v128, v128, v130\n\t"
      "v_add_f32 v129, v129, v131\n\t"
      "v_add_f32 v132, v132, v134\n\t"
      "v_add_f32 v133, v133, v135\n\t"
      "v_cvt_pk_bf16_f32 v152, v128, v129\n\t"
      "ds_write2_b32 %[tro], v152, v152 offset0:0 offset1:1\n\t"
      "v_cvt_pk_bf16_f32 v153, v132, v133\n\t"
      "ds_write2_b32 %[tro], v153, v153 offset0:128 offset1:129\n\t"
      "v_smfmac_f32_16x16x64_bf16 v[136:139], v[36:39], v[176:183], %[idx]\n\t"
      "v_smfmac_f32_16x16x64_bf16 v[140:143], v[36:39], v[184:191], %[idx]\n\t"
      "s_cmp_eq_u32 s40, 63\n\t"
      "v_smfmac_f32_16x16x64_bf16 v[136:139], v[48:51], v[192:199], %[idx]\n\t"
      "ds_read_b128 v[176:179], %[wvrd] offset:4096\n\t"
      "v_smfmac_f32_16x16x64_bf16 v[140:143], v[48:51], v[200:207], %[idx]\n\t"
      "ds_read_b128 v[180:183], %[wvrd] offset:5120\n\t"
      "v_smfmac_f32_16x16x64_bf16 v[136:139], v[52:55], v[208:215], %[idx]\n\t"
      "s_waitcnt lgkmcnt(2)\n\t"
      "s_barrier\n\t"
      "s_cbranch_scc1 L_last_%=\n\t"
      "ds_read_b64_tr_b16 v[104:105], %[xrd] offset:0\n\t"
      "ds_read_b64_tr_b16 v[106:107], %[xrd] offset:512\n\t"
      "ds_read_b64_tr_b16 v[108:109], %[xrd] offset:1024\n\t"
      "ds_read_b64_tr_b16 v[110:111], %[xrd] offset:1536\n\t"
      "s_mov_b32 m0, s47\n\t"
      "v_smfmac_f32_16x16x64_bf16 v[140:143], v[52:55], v[216:223], %[idx]\n\t"
      "ds_read_b64_tr_b16 v[112:113], %[xrd] offset:2048\n\t"
      "ds_read_b64_tr_b16 v[114:115], %[xrd] offset:2560\n\t"
      "v_smfmac_f32_16x16x64_bf16 v[136:139], v[56:59], v[224:231], %[idx]\n\t"
      "ds_read_b64_tr_b16 v[116:117], %[xrd] offset:3072\n\t"
      "ds_read_b64_tr_b16 v[118:119], %[xrd] offset:3584\n\t"
      "s_waitcnt lgkmcnt(6)\n\t"
      "v_mfma_f32_16x16x32_bf16 v[124:127], v[80:83], v[104:107], 0\n\t"
      "ds_read_b64_tr_b16 v[160:161], %[xrd] offset:4096\n\t"
      "ds_read_b64_tr_b16 v[162:163], %[xrd] offset:4608\n\t"
      "s_waitcnt lgkmcnt(6)\n\t"
      "v_mfma_f32_16x16x32_bf16 v[124:127], v[84:87], v[108:111], v[124:127]\n\t"
      "ds_read_b64_tr_b16 v[164:165], %[xrd] offset:5120\n\t"
      "ds_read_b64_tr_b16 v[166:167], %[xrd] offset:5632\n\t"
      "s_waitcnt lgkmcnt(6)\n\t"
      "v_mfma_f32_16x16x32_bf16 v[124:127], v[88:91], v[112:115], v[124:127]\n\t"
      "ds_read_b64_tr_b16 v[168:169], %[xrd] offset:6144\n\t"
      "ds_read_b64_tr_b16 v[170:171], %[xrd] offset:6656\n\t"
      "s_waitcnt lgkmcnt(6)\n\t"
      "v_mfma_f32_16x16x32_bf16 v[124:127], v[92:95], v[116:119], v[124:127]\n\t"
      "ds_read_b64_tr_b16 v[172:173], %[xrd] offset:7168\n\t"
      "ds_read_b64_tr_b16 v[174:175], %[xrd] offset:7680\n\t"
      "v_mfma_f32_16x16x32_bf16 v[120:123], v[64:67], v[104:107], 0\n\t"
      "ds_read_b128 v[184:187], %[wvrd] offset:12288\n\t"
      "ds_read_b128 v[188:191], %[wvrd] offset:13312\n\t"
      "v_mfma_f32_16x16x32_bf16 v[120:123], v[68:71], v[108:111], v[120:123]\n\t"
      "ds_read_b128 v[80:83], %[wvrd] offset:6144\n\t"
      "ds_read_b128 v[84:87], %[wvrd] offset:7168\n\t"
      "v_mfma_f32_16x16x32_bf16 v[120:123], v[72:75], v[112:115], v[120:123]\n\t"
      "ds_read_b128 v[88:91], %[wvrd] offset:14336\n\t"
      "ds_read_b128 v[92:95], %[wvrd] offset:15360\n\t"
      "v_mfma_f32_16x16x32_bf16 v[120:123], v[76:79], v[116:119], v[120:123]\n\t"
      "buffer_load_dwordx4 %[memrd], s[60:63], s44 offen sc1 lds\n\t"
      "v_exp_f32 v124, v124\n\t"
      "s_waitcnt lgkmcnt(12)\n\t"
      "v_mfma_f32_16x16x32_bf16 v[144:147], v[160:163], v[104:107], 0\n\t"
      "ds_read_b128 v[64:67], %[wvrd] offset:16384\n\t"
      "buffer_load_dwordx4 %[memrd], s[60:63], s44 offen offset:1024 sc1 lds\n\t"
      "s_waitcnt lgkmcnt(11)\n\t"
      "v_mfma_f32_16x16x32_bf16 v[144:147], v[164:167], v[108:111], v[144:147]\n\t"
      "v_exp_f32 v125, v125\n\t"
      "s_waitcnt lgkmcnt(9)\n\t"
      "v_mfma_f32_16x16x32_bf16 v[144:147], v[168:171], v[112:115], v[144:147]\n\t"
      "ds_read_b128 v[68:71], %[wvrd] offset:17408\n\t"
      "s_mov_b32 m0, s48\n\t"
      "s_waitcnt lgkmcnt(8)\n\t"
      "v_mfma_f32_16x16x32_bf16 v[144:147], v[172:175], v[116:119], v[144:147]\n\t"
      "v_exp_f32 v126, v126\n\t"
      "s_barrier\n\t"
      "v_smfmac_f32_16x16x64_bf16 v[136:139], v[40:43], v[176:183], %[idx]\n\t"
      "ds_read_b128 v[72:75], %[wvrd] offset:20480\n\t"
      "s_waitcnt lgkmcnt(7)\n\t"
      "v_smfmac_f32_16x16x64_bf16 v[140:143], v[40:43], v[184:191], %[idx]\n\t"
      "v_exp_f32 v127, v127\n\t"
      "buffer_load_dwordx4 %[memrd], s[60:63], s45 offen sc1 lds\n\t"
      "s_waitcnt lgkmcnt(5)\n\t"
      "v_smfmac_f32_16x16x64_bf16 v[136:139], v[44:47], v[80:87], %[idx]\n\t"
      "ds_read_b128 v[76:79], %[wvrd] offset:21504\n\t"
      "v_exp_f32 v120, v120\n\t"
      "s_waitcnt lgkmcnt(4)\n\t"
      "v_smfmac_f32_16x16x64_bf16 v[140:143], v[44:47], v[88:95], %[idx]\n\t"
      "ds_read_b128 v[176:179], %[wvrd] offset:18432\n\t"
      "v_exp_f32 v121, v121\n\t"
      "v_smfmac_f32_16x16x64_bf16 v[140:143], v[56:59], v[232:239], %[idx]\n\t"
      "ds_read_b128 v[180:183], %[wvrd] offset:19456\n\t"
      "v_exp_f32 v122, v122\n\t"
      "v_smfmac_f32_16x16x64_bf16 v[136:139], v[60:63], v[240:247], %[idx]\n\t"
      "ds_read_b128 v[184:187], %[wvrd] offset:22528\n\t"
      "v_exp_f32 v123, v123\n\t"
      "v_smfmac_f32_16x16x64_bf16 v[140:143], v[60:63], v[248:255], %[idx]\n\t"
      "ds_read_b128 v[188:191], %[wvrd] offset:23552\n\t"
      "v_cvt_pk_bf16_f32 v154, v124, v125\n\t"
      "v_cvt_pk_bf16_f32 v155, v126, v127\n\t"
      "v_cvt_pk_bf16_f32 v152, v120, v121\n\t"
      "v_cvt_pk_bf16_f32 v153, v122, v123\n\t"
      "v_exp_f32 v144, v144\n\t"
      "buffer_load_dwordx4 %[memrd], s[60:63], s45 offen offset:1024 sc1 lds\n\t"
      "v_mfma_f32_16x16x32_bf16 v[128:131], v[96:99], v[152:155], 0\n\t"
      "v_exp_f32 v145, v145\n\t"
      "v_mfma_f32_16x16x32_bf16 v[132:135], v[100:103], v[152:155], 0\n\t"
      "v_cvt_pk_bf16_f32 v120, v136, v137\n\t"
      "v_cvt_pk_bf16_f32 v156, v144, v145\n\t"
      "v_mfma_f32_16x16x32_bf16 v[148:151], v[152:155], %[ones], 0\n\t"
      "v_cvt_pk_bf16_f32 v157, v144, v145\n\t"
      "v_cvt_pk_bf16_f32 v121, v138, v139\n\t"
      "v_cvt_pk_bf16_f32 v122, v140, v141\n\t"
      "s_nop 0\n\t"
      "v_mfma_f32_16x16x16_bf16 v[128:131], v[120:121], v[156:157], v[128:131]\n\t"
      "v_cvt_pk_bf16_f32 v123, v142, v143\n\t"
      "ds_read_b128 v[160:163], %[wvrd] offset:24576\n\t"
      "s_nop 0\n\t"
      "v_mfma_f32_16x16x16_bf16 v[132:135], v[122:123], v[156:157], v[132:135]\n\t"
      "ds_read_b128 v[164:167], %[wvrd] offset:25600\n\t"
      "ds_read_b128 v[168:171], %[wvrd] offset:28672\n\t"
      "v_mfma_f32_16x16x16_bf16 v[148:151], v[156:157], %[onesm], v[148:151]\n\t"
      "ds_read_b128 v[172:175], %[wvrd] offset:29696\n\t"
      "v_cvt_pk_bf16_f32 v152, v128, v129\n\t"
      "v_cvt_pk_bf16_f32 v153, v130, v131\n\t"
      "s_mov_b64 exec, s[58:59]\n\t"
      "s_nop 0\n\t"
      "v_cvt_pk_bf16_f32 v152, v132, v133\n\t"
      "v_cvt_pk_bf16_f32 v153, v134, v135\n\t"
      "s_mov_b64 exec, -1\n\t"
      "ds_write_b64 %[owr], v[152:153]\n\t"
      "v_subrev_u32 v144, s56, v148\n\t"
      "v_subrev_u32 v145, s56, v149\n\t"
      "v_max_u32 v144, v144, v145\n\t"
      "v_cmp_lt_u32 vcc, s57, v144\n\t"
      "v_mov_b64 v[40:41], 0\n\t"
      "v_mov_b64 v[42:43], 0\n\t"
      "v_mov_b64 v[44:45], 0\n\t"
      "v_mov_b64 v[46:47], 0\n\t"
      "v_mov_b64 v[48:49], 0\n\t"
      "v_mov_b64 v[50:51], 0\n\t"
      "v_mov_b64 v[52:53], 0\n\t"
      "v_mov_b64 v[54:55], 0\n\t"
      "s_waitcnt lgkmcnt(0)\n\t"
      "s_barrier\n\t"
      "L_back_%=:\n\t"
      "ds_read_b128 v[32:35], %[memrd] offset:24576\n\t"
      "ds_read_b128 v[36:39], %[memrd] offset:25600\n\t"
      "s_cbranch_vccnz L_slow_%=\n\t"
      "ds_read_b128 v[80:83], %[wvrd] offset:26624\n\t"
      "ds_read_b128 v[84:87], %[wvrd] offset:27648\n\t"
      "ds_read_b128 v[88:91], %[wvrd] offset:30720\n\t"
      "ds_read_b128 v[92:95], %[wvrd] offset:31744\n\t"
      "v_rcp_f32 v148, v148\n\t"
      "v_rcp_f32 v149, v149\n\t"
      "s_waitcnt lgkmcnt(5)\n\t"
      "v_smfmac_f32_16x16x64_bf16 v[40:43], v[32:35], v[64:71], %[idx]\n\t"
      "v_smfmac_f32_16x16x64_bf16 v[44:47], v[32:35], v[72:79], %[idx]\n\t"
      "s_waitcnt lgkmcnt(4)\n\t"
      "v_smfmac_f32_16x16x64_bf16 v[40:43], v[36:39], v[176:183], %[idx]\n\t"
      "v_smfmac_f32_16x16x64_bf16 v[44:47], v[36:39], v[184:191], %[idx]\n\t"
      "v_smfmac_f32_16x16x64_bf16 v[48:51], v[32:35], v[160:167], %[idx]\n\t"
      "s_nop 1\n\t"
      "v_smfmac_f32_16x16x64_bf16 v[52:55], v[32:35], v[168:175], %[idx]\n\t"
      "s_nop 1\n\t"
      "s_waitcnt lgkmcnt(2)\n\t"
      "v_smfmac_f32_16x16x64_bf16 v[48:51], v[36:39], v[80:87], %[idx]\n\t"
      "v_add_f32 v40, v40, v42\n\t"
      "s_waitcnt lgkmcnt(0)\n\t"
      "v_smfmac_f32_16x16x64_bf16 v[52:55], v[36:39], v[88:95], %[idx]\n\t"
      "v_add_f32 v41, v41, v43\n\t"
      "v_add_f32 v44, v44, v46\n\t"
      "v_add_f32 v45, v45, v47\n\t"
      "v_mul_f32 v40, v40, v148\n\t"
      "v_mul_f32 v41, v41, v149\n\t"
      "v_mul_f32 v44, v44, v148\n\t"
      "v_mul_f32 v45, v45, v149\n\t"
      "v_cvt_pk_bf16_f32 v152, v40, v44\n\t"
      "v_cvt_pk_bf16_f32 v154, v41, v45\n\t"
      "v_pk_add_f32 v[48:49], v[48:49], v[50:51]\n\t"
      "v_pk_add_f32 v[52:53], v[52:53], v[54:55]\n\t"
      "v_pk_mul_f32 v[48:49], v[48:49], v[148:149]\n\t"
      "v_pk_mul_f32 v[52:53], v[52:53], v[148:149]\n\t"
      "v_cvt_pk_bf16_f32 v153, v48, v52\n\t"
      "v_cvt_pk_bf16_f32 v155, v49, v53\n\t"
      "buffer_store_dwordx4 v[152:155], %[pst], s[96:99], s42 offen sc1\n\t"
      "s_cmp_eq_u32 s93, 0\n\t"
      "s_cbranch_scc0 L_pubva_%=\n\t"
      "v_add_u32 v120, s66, %[xrd]\n\t"
      "ds_read_b64_tr_b16 v[156:157], v120 offset:4096\n\t"
      "ds_read_b64_tr_b16 v[122:123], v120 offset:4608\n\t"
      "v_cmp_ne_u32 vcc, 0x4444, %[idx]\n\t"
      "s_waitcnt lgkmcnt(0)\n\t"
      "s_nop 0\n\t"
      "v_cndmask_b32 v156, v156, v122, vcc\n\t"
      "v_cndmask_b32 v157, v157, v123, vcc\n\t"
      "s_branch L_puba_%=\n\t"
      "L_pubva_%=:\n\t"
      "s_nop 7\n\t"
      "v_add_f32 v136, v136, v138\n\t"
      "v_add_f32 v137, v137, v139\n\t"
      "v_add_f32 v140, v140, v142\n\t"
      "v_add_f32 v141, v141, v143\n\t"
      "v_cvt_pk_bf16_f32 v156, v136, v137\n\t"
      "v_cvt_pk_bf16_f32 v157, v140, v141\n\t"
      "L_puba_%=:\n\t"
      "v_mov_b32 v144, -1\n\t"
      "v_mov_b32 v145, -1\n\t"
      "v_mov_b32 v146, -1\n\t"
      "v_mov_b32 v147, -1\n\t"
      "s_waitcnt vmcnt(1)\n\t"
      "v_pk_min_u16 v120, v158, v159\n\t"
      "v_readfirstlane_b32 s32, v158\n\t"
      "v_readfirstlane_b32 s33, v159\n\t"
      "v_readfirstlane_b32 s53, v120\n\t"
      "s_nop 2\n\t"
      "s_lshr_b64 s[34:35], s[32:33], s50\n\t"
      "s_and_b32 s34, s34, 0xffff\n\t"
      "s_lshr_b32 s32, s53, 16\n\t"
      "s_and_b32 s53, s53, 0xffff\n\t"
      "s_min_u32 s53, s53, s32\n\t"
      "s_cmp_eq_u32 s34, 0xffff\n\t"
      "s_cselect_b32 s34, 22528, s34\n\t"
      "s_cselect_b32 s53, s34, s53\n\t"
      "s_sub_u32 s53, s34, s53\n\t"
      "s_mul_i32 s53, s53, " HSTR(CLK_K) "\n\t"
      "s_mul_i32 s32, s34, " HSTR(CLK_T1) "\n\t"
      "s_mul_i32 s72, s34, " HSTR(CLK_T2) "\n\t"
      "s_add_u32 s53, s53, s32\n\t"
      "s_add_u32 s53, s53, " HSTR(CLK_O1) "\n\t"
      "s_add_u32 s72, s72, " HSTR(CLK_O2) "\n\t"
      "s_max_i32 s53, s53, 0x10000\n\t"
      "s_max_i32 s72, s72, 0x10000\n\t"
      "s_lshr_b32 s70, s53, 16\n\t"
      "s_lshr_b32 s72, s72, 16\n\t"
      "s_min_u32 s70, s70, 40\n\t"
      "s_min_u32 s72, s72, 40\n\t"
      "L_d1_%=:\n\t"
      "s_sub_u32 s70, s70, 1\n\t"
      "s_cmp_lg_u32 s70, 0\n\t"
      "s_cbranch_scc1 L_d1_%=\n\t"
      "L_poll1_%=:\n\t"
      "buffer_load_dwordx4 v[112:115], %[pld], s[96:99], s42 offen sc1\n\t"
      "s_waitcnt vmcnt(0)\n\t"
      "v_mfma_f32_16x16x32_bf16 v[108:111], v[112:115], %[selb], 0\n\t"
      "v_max3_u32 v158, v112, v113, v114\n\t"
      "v_max_u32 v158, v158, v115\n\t"
      "v_cmp_eq_u32 vcc, -1, v158\n\t"
      "s_cbranch_vccnz L_retry1_%=\n\t"
      "L_red_%=:\n\t"
      "s_nop 3\n\t"
      "v_add_f32 v108, v108, v109\n\t"
      "v_add_f32 v110, v110, v111\n\t"
      "v_add_f32 v108, v108, v110\n\t"
      "v_mov_b32 v109, v108\n\t"
      "s_nop 1\n\t"
      "v_permlane16_swap_b32 v108, v109\n\t"
      "v_add_f32 v108, v108, v109\n\t"
      "s_nop 1\n\t"
      "v_mov_b32_dpp v109, v108 row_shl:1 row_mask:0xf bank_mask:0xf\n\t"
      "v_cvt_pk_bf16_f32 v108, v108, v109\n\t"
      "buffer_store_dword v108, %[mst], s[96:99], s43 offen sc1\n\t"
      "buffer_store_dwordx4 v[144:147], %[pld], s[96:99], s42 offen sc1\n\t"
      "s_add_u32 s46, s46, 131072\n\t"
      "buffer_store_dwordx2 v[156:157], %[kvp], s[96:99], s46 offen sc1\n\t"
      "v_readfirstlane_b32 s52, %[qkv]\n\t"
      "s_sub_u32 s54, s40, 0\n\t"
      "v_mov_b32 %[oe], s54\n\t"
      "s_add_u32 s40, s40, 1\n\t"
      "s_cmp_eq_u32 s62, 0\n\t"
      "s_cbranch_scc1 L_kvlate_%=\n\t"
      "L_kvok_%=:\n\t"
      "s_add_u32 s41, s41, 16384\n\t"
      "s_xor_b32 s42, s42, 524288\n\t"
      "s_add_u32 s43, s43, 16384\n\t"
      "s_add_u32 s44, s44, 524288\n\t"
      "s_add_u32 s45, s45, 524288\n\t"
      "s_add_u32 s46, s46, 0\n\t"
      "s_add_u32 s95, s40, 1\n\t"
      "s_min_u32 s95, s95, 62\n\t"
      "s_cmp_ge_i32 s52, s95\n\t"
      "s_cselect_b32 s62, s98, 0\n\t"
      "ds_read_b128 v[160:163], %[wvrd] offset:0\n\t"
      "ds_read_b128 v[164:167], %[wvrd] offset:1024\n\t"
      "ds_read_b128 v[168:171], %[wvrd] offset:8192\n\t"
      "ds_read_b128 v[172:175], %[wvrd] offset:9216\n\t"
      "ds_read_b128 v[176:179], %[wvrd] offset:2048\n\t"
      "ds_read_b128 v[180:183], %[wvrd] offset:3072\n\t"
      "ds_read_b128 v[184:187], %[wvrd] offset:10240\n\t"
      "ds_read_b128 v[188:191], %[wvrd] offset:11264\n\t"
      "v_mov_b64 v[120:121], 0\n\t"
      "v_mov_b64 v[122:123], 0\n\t"
      "v_mov_b64 v[124:125], 0\n\t"
      "v_mov_b64 v[126:127], 0\n\t"
      "v_mov_b64 v[128:129], 0\n\t"
      "v_mov_b64 v[130:131], 0\n\t"
      "v_mov_b64 v[132:133], 0\n\t"
      "v_mov_b64 v[134:135], 0\n\t"
      "v_mov_b64 v[136:137], 0\n\t"
      "v_mov_b64 v[138:139], 0\n\t"
      "v_mov_b64 v[140:141], 0\n\t"
      "v_mov_b64 v[142:143], 0\n\t"
      "s_and_b32 s53, s40, " HSTR(CLK_MASK) "\n\t"
      "s_or_b32 s53, s53, s65\n\t"
      "s_cmp_eq_u32 s53, 0\n\t"
      "s_cbranch_scc1 L_clk_%=\n\t"
      "L_clkb_%=:\n\t"
      "s_mov_b32 s70, s72\n\t"
      "L_d2_%=:\n\t"
      "s_sub_u32 s70, s70, 1\n\t"
      "s_cmp_lg_u32 s70, 0\n\t"
      "s_cbranch_scc1 L_d2_%=\n\t"
      "s_setpc_b64 s[38:39]\n\t"
      "L_retry2_%=:\n\t"
      "s_add_u32 s49, s49, 1\n\t"
      "s_sleep " HSTR(RB_UNITS) "\n\t"
      "v_mov_b64 v[120:121], 0\n\t"
      "v_mov_b64 v[122:123], 0\n\t"
      "v_mov_b64 v[124:125], 0\n\t"
      "v_mov_b64 v[126:127], 0\n\t"
      "s_cmp_lt_u32 s49, 100000\n\t"
      "s_cbranch_scc1 L_r2_%=\n\t"
      "s_mov_b64 s[68:69], 0\n\t"
      "s_mov_b32 s53, 2118656\n\t"
      "v_mov_b32 v159, 1\n\t"
      "buffer_store_dword v159, off, s[96:99], s53 sc1\n\t"
      "L_r2_%=:\n\t"
      "s_setpc_b64 s[38:39]\n\t"
      "L_retry1_%=:\n\t"
      "s_add_u32 s49, s49, 1\n\t"
      "s_sleep " HSTR(RB_UNITS) "\n\t"
      "s_cmp_lt_u32 s49, 100000\n\t"
      "s_cbranch_scc1 L_poll1_%=\n\t"
      "s_mov_b32 s53, 2118656\n\t"
      "v_mov_b32 v159, 1\n\t"
      "buffer_store_dword v159, off, s[96:99], s53 sc1\n\t"
      "s_branch L_red_%=\n\t"
      "L_clk_%=:\n\t"
      "s_memtime s[32:33]\n\t"
      "s_memrealtime s[34:35]\n\t"
      "s_waitcnt lgkmcnt(0)\n\t"
      "s_sub_u32 s33, s32, s28\n\t"
      "s_sub_u32 s35, s34, s29\n\t"
      "s_mov_b32 s28, s36\n\t"
      "s_mov_b32 s29, s37\n\t"
      "s_mov_b32 s36, s32\n\t"
      "s_mov_b32 s37, s34\n\t"
      "v_cvt_f32_u32 v158, s35\n\t"
      "v_cvt_f32_u32 v159, s33\n\t"
      "v_rcp_f32 v158, v158\n\t"
      "v_mul_f32 v159, 0x44800000, v159\n\t"
      "v_mul_f32 v158, v158, v159\n\t"
      "v_cvt_u32_f32 v158, v158\n\t"
      "s_mov_b32 s27, 1\n\t"
      "s_sub_u32 s72, s72, 4\n\t"
      "s_max_i32 s72, s72, 1\n\t"
      "s_branch L_clkb_%=\n\t"
      "L_clk0_%=:\n\t"
      "s_memtime s[32:33]\n\t"
      "s_memrealtime s[34:35]\n\t"
      "s_waitcnt lgkmcnt(0)\n\t"
      "s_sub_u32 s33, s32, s28\n\t"
      "s_sub_u32 s35, s34, s29\n\t"
      "s_mov_b32 s28, s36\n\t"
      "s_mov_b32 s29, s37\n\t"
      "s_mov_b32 s36, s32\n\t"
      "s_mov_b32 s37, s34\n\t"
      "v_cvt_f32_u32 v158, s35\n\t"
      "v_cvt_f32_u32 v159, s33\n\t"
      "v_rcp_f32 v158, v158\n\t"
      "v_mul_f32 v159, 0x44800000, v159\n\t"
      "v_mul_f32 v158, v158, v159\n\t"
      "v_cvt_u32_f32 v158, v158\n\t"
      "buffer_store_short v158, off, s[96:99], s30 sc1\n\t"
      "s_branch L_clk0b_%=\n\t"
      "L_cst0_%=:\n\t"
      "buffer_store_short v158, %[trq], s[96:99], s30 offen sc1\n\t"
      "s_mov_b32 s27, 0\n\t"
      "s_branch L_cstb0_%=\n\t"
      "L_cst1_%=:\n\t"
      "buffer_store_short v158, %[trq], s[96:99], s30 offen sc1\n\t"
      "s_mov_b32 s27, 0\n\t"
      "s_branch L_cstb1_%=\n\t"
      "L_cst2_%=:\n\t"
      "buffer_store_short v158, %[trq], s[96:99], s30 offen sc1\n\t"
      "s_mov_b32 s27, 0\n\t"
      "s_branch L_cstb2_%=\n\t"
      "L_cst3_%=:\n\t"
      "buffer_store_short v158, %[trq], s[96:99], s30 offen sc1\n\t"
      "s_mov_b32 s27, 0\n\t"
      "s_branch L_cstb3_%=\n\t"
      "L_kvlate_%=:\n\t"
      "s_min_u32 s95, s40, 62\n\t"
      "L_kvs_%=:\n\t"
      "buffer_load_dword v159, off, s[96:99], s55 sc1\n\t"
      "s_waitcnt vmcnt(0)\n\t"
      "v_readfirstlane_b32 s52, v159\n\t"
      "s_nop 3\n\t"
      "s_cmp_ge_i32 s52, s95\n\t"
      "s_cbranch_scc1 L_kvs_done_%=\n\t"
      "s_add_u32 s49, s49, 1\n\t"
      "s_cmp_lt_u32 s49, 100000\n\t"
      "s_cbranch_scc1 L_kvs_%=\n\t"
      "s_mov_b32 s53, 2118656\n\t"
      "v_mov_b32 v159, 1\n\t"
      "buffer_store_dword v159, off, s[96:99], s53 sc1\n\t"
      "L_kvs_done_%=:\n\t"
      "s_mov_b32 m0, s47\n\t"
      "s_nop 0\n\t"
      "buffer_load_dwordx4 %[memrd], s[96:99], s44 offen sc1 lds\n\t"
      "buffer_load_dwordx4 %[memrd], s[96:99], s44 offen offset:1024 sc1 lds\n\t"
      "s_mov_b32 m0, s48\n\t"
      "s_nop 0\n\t"
      "buffer_load_dwordx4 %[memrd], s[96:99], s45 offen sc1 lds\n\t"
      "buffer_load_dwordx4 %[memrd], s[96:99], s45 offen offset:1024 sc1 lds\n\t"
      "s_branch L_kvok_%=\n\t"
      "L_slow_%=:\n\t"
      "s_waitcnt lgkmcnt(0)\n\t"
      "s_barrier\n\t"
      "s_nop 7\n\t"
      "s_lshl_b32 s53, s94, 11\n\t"
      "s_sub_u32 s52, s44, s53\n\t"
      "s_sub_u32 s52, s52, 524288\n\t"
      "buffer_load_dwordx4 v[32:35], %[memrd], s[96:99], s52 offen offset:0 sc1\n\t"
      "buffer_load_dwordx4 v[36:39], %[memrd], s[96:99], s52 offen offset:1024 sc1\n\t"
      "buffer_load_dwordx4 v[56:59], %[memrd], s[96:99], s52 offen offset:2048 sc1\n\t"
      "buffer_load_dwordx4 v[60:63], %[memrd], s[96:99], s52 offen offset:3072 sc1\n\t"
      "s_add_u32 s52, s52, 4096\n\t"
      "s_waitcnt vmcnt(0)\n\t"
      "v_mfma_f32_16x16x32_bf16 v[120:123], v[32:35], v[104:107], 0\n\t"
      "v_mfma_f32_16x16x32_bf16 v[120:123], v[36:39], v[108:111], v[120:123]\n\t"
      "v_mfma_f32_16x16x32_bf16 v[120:123], v[56:59], v[112:115], v[120:123]\n\t"
      "v_mfma_f32_16x16x32_bf16 v[120:123], v[60:63], v[116:119], v[120:123]\n\t"
      "s_nop 7\n\t"
      "buffer_load_dwordx4 v[32:35], %[memrd], s[96:99], s52 offen offset:0 sc1\n\t"
      "buffer_load_dwordx4 v[36:39], %[memrd], s[96:99], s52 offen offset:1024 sc1\n\t"
      "buffer_load_dwordx4 v[56:59], %[memrd], s[96:99], s52 offen offset:2048 sc1\n\t"
      "buffer_load_dwordx4 v[60:63], %[memrd], s[96:99], s52 offen offset:3072 sc1\n\t"
      "s_add_u32 s52, s52, 4096\n\t"
      "s_waitcnt vmcnt(0)\n\t"
      "v_mfma_f32_16x16x32_bf16 v[124:127], v[32:35], v[104:107], 0\n\t"
      "v_mfma_f32_16x16x32_bf16 v[124:127], v[36:39], v[108:111], v[124:127]\n\t"
      "v_mfma_f32_16x16x32_bf16 v[124:127], v[56:59], v[112:115], v[124:127]\n\t"
      "v_mfma_f32_16x16x32_bf16 v[124:127], v[60:63], v[116:119], v[124:127]\n\t"
      "s_nop 7\n\t"
      "ds_read_b64_tr_b16 v[32:33], %[xrd] offset:4096\n\t"
      "ds_read_b64_tr_b16 v[34:35], %[xrd] offset:4608\n\t"
      "ds_read_b64_tr_b16 v[36:37], %[xrd] offset:5120\n\t"
      "ds_read_b64_tr_b16 v[38:39], %[xrd] offset:5632\n\t"
      "ds_read_b64_tr_b16 v[56:57], %[xrd] offset:6144\n\t"
      "ds_read_b64_tr_b16 v[58:59], %[xrd] offset:6656\n\t"
      "ds_read_b64_tr_b16 v[60:61], %[xrd] offset:7168\n\t"
      "ds_read_b64_tr_b16 v[62:63], %[xrd] offset:7680\n\t"
      "s_waitcnt lgkmcnt(6)\n\t"
      "v_mfma_f32_16x16x32_bf16 v[144:147], v[32:35], v[104:107], 0\n\t"
      "s_waitcnt lgkmcnt(4)\n\t"
      "v_mfma_f32_16x16x32_bf16 v[144:147], v[36:39], v[108:111], v[144:147]\n\t"
      "s_waitcnt lgkmcnt(2)\n\t"
      "v_mfma_f32_16x16x32_bf16 v[144:147], v[56:59], v[112:115], v[144:147]\n\t"
      "s_waitcnt lgkmcnt(0)\n\t"
      "v_mfma_f32_16x16x32_bf16 v[144:147], v[60:63], v[116:119], v[144:147]\n\t"
      "v_max3_f32 v156, v120, v121, v122\n\t"
      "v_max3_f32 v156, v156, v123, v124\n\t"
      "v_max3_f32 v156, v156, v125, v126\n\t"
      "s_nop 4\n\t"
      "v_max3_f32 v156, v156, v127, v144\n\t"
      "v_max_f32 v156, v156, v145\n\t"
      "v_mov_b32 v157, v156\n\t"
      "s_nop 1\n\t"
      "v_permlane16_swap_b32 v156, v157\n\t"
      "v_max_f32 v156, v156, v157\n\t"
      "v_mov_b32 v157, v156\n\t"
      "s_nop 1\n\t"
      "v_permlane32_swap_b32 v156, v157\n\t"
      "v_max_f32 v156, v156, v157\n\t"
      "v_sub_f32 v120, v120, v156\n\t"
      "v_sub_f32 v121, v121, v156\n\t"
      "v_sub_f32 v122, v122, v156\n\t"
      "v_sub_f32 v123, v123, v156\n\t"
      "v_sub_f32 v124, v124, v156\n\t"
      "v_sub_f32 v125, v125, v156\n\t"
      "v_sub_f32 v126, v126, v156\n\t"
      "v_sub_f32 v127, v127, v156\n\t"
      "v_sub_f32 v144, v144, v156\n\t"
      "v_sub_f32 v145, v145, v156\n\t"
      "v_exp_f32 v120, v120\n\t"
      "v_exp_f32 v121, v121\n\t"
      "v_exp_f32 v122, v122\n\t"
      "v_exp_f32 v123, v123\n\t"
      "v_exp_f32 v124, v124\n\t"
      "v_exp_f32 v125, v125\n\t"
      "v_exp_f32 v126, v126\n\t"
      "v_exp_f32 v127, v127\n\t"
      "v_exp_f32 v144, v144\n\t"
      "v_exp_f32 v145, v145\n\t"
      "v_cvt_pk_bf16_f32 v152, v120, v121\n\t"
      "v_cvt_pk_bf16_f32 v153, v122, v123\n\t"
      "v_cvt_pk_bf16_f32 v154, v124, v125\n\t"
      "v_cvt_pk_bf16_f32 v155, v126, v127\n\t"
      "v_cvt_pk_bf16_f32 v156, v144, v145\n\t"
      "v_cvt_pk_bf16_f32 v157, v144, v145\n\t"
      "v_cvt_pk_bf16_f32 v120, v136, v137\n\t"
      "v_cvt_pk_bf16_f32 v121, v138, v139\n\t"
      "v_cvt_pk_bf16_f32 v122, v140, v141\n\t"
      "v_cvt_pk_bf16_f32 v123, v142, v143\n\t"
      "v_mfma_f32_16x16x32_bf16 v[128:131], v[96:99], v[152:155], 0\n\t"
      "v_mfma_f32_16x16x32_bf16 v[132:135], v[100:103], v[152:155], 0\n\t"
      "v_mfma_f32_16x16x32_bf16 v[148:151], v[152:155], %[ones], 0\n\t"
      "s_nop 3\n\t"
      "v_mfma_f32_16x16x16_bf16 v[128:131], v[120:121], v[156:157], v[128:131]\n\t"
      "v_mfma_f32_16x16x16_bf16 v[132:135], v[122:123], v[156:157], v[132:135]\n\t"
      "v_mfma_f32_16x16x16_bf16 v[148:151], v[156:157], %[onesm], v[148:151]\n\t"
      "s_nop 5\n\t"
      "v_cvt_pk_bf16_f32 v152, v128, v129\n\t"
      "v_cvt_pk_bf16_f32 v153, v130, v131\n\t"
      "s_mov_b64 exec, s[58:59]\n\t"
      "v_cvt_pk_bf16_f32 v152, v132, v133\n\t"
      "v_cvt_pk_bf16_f32 v153, v134, v135\n\t"
      "s_mov_b64 exec, -1\n\t"
      "ds_write_b64 %[owr], v[152:153]\n\t"
      "s_mov_b64 vcc, 0\n\t"
      "s_waitcnt lgkmcnt(0)\n\t"
      "s_barrier\n\t"
      "s_branch L_back_%=\n\t"
      "L_last_%=:\n\t"
      "s_cmp_eq_u32 s93, 0\n\t"
      "s_cbranch_scc1 L_lastk_%=\n\t"
      "ds_read_b128 v[184:187], %[wvrd] offset:12288\n\t"
      "ds_read_b128 v[188:191], %[wvrd] offset:13312\n\t"
      "ds_read_b128 v[160:163], %[wvrd] offset:6144\n\t"
      "ds_read_b128 v[164:167], %[wvrd] offset:7168\n\t"
      "ds_read_b128 v[168:171], %[wvrd] offset:14336\n\t"
      "ds_read_b128 v[172:175], %[wvrd] offset:15360\n\t"
      "v_smfmac_f32_16x16x64_bf16 v[140:143], v[52:55], v[216:223], %[idx]\n\t"
      "v_smfmac_f32_16x16x64_bf16 v[136:139], v[56:59], v[224:231], %[idx]\n\t"
      "v_smfmac_f32_16x16x64_bf16 v[140:143], v[56:59], v[232:239], %[idx]\n\t"
      "v_smfmac_f32_16x16x64_bf16 v[136:139], v[60:63], v[240:247], %[idx]\n\t"
      "v_smfmac_f32_16x16x64_bf16 v[140:143], v[60:63], v[248:255], %[idx]\n\t"
      "s_waitcnt lgkmcnt(0)\n\t"
      "v_smfmac_f32_16x16x64_bf16 v[136:139], v[40:43], v[176:183], %[idx]\n\t"
      "v_smfmac_f32_16x16x64_bf16 v[140:143], v[40:43], v[184:191], %[idx]\n\t"
      "v_smfmac_f32_16x16x64_bf16 v[136:139], v[44:47], v[160:167], %[idx]\n\t"
      "v_smfmac_f32_16x16x64_bf16 v[140:143], v[44:47], v[168:175], %[idx]\n\t"
      "L_lastk_%=:\n\t"
      "s_nop 7\n\t"
      "s_cmp_eq_u32 s93, 0\n\t"
      "s_cbranch_scc0 L_pubvb_%=\n\t"
      "v_add_u32 v120, s66, %[xrd]\n\t"
      "ds_read_b64_tr_b16 v[156:157], v120 offset:4096\n\t"
      "ds_read_b64_tr_b16 v[122:123], v120 offset:4608\n\t"
      "v_cmp_ne_u32 vcc, 0x4444, %[idx]\n\t"
      "s_waitcnt lgkmcnt(0)\n\t"
      "s_nop 0\n\t"
      "v_cndmask_b32 v156, v156, v122, vcc\n\t"
      "v_cndmask_b32 v157, v157, v123, vcc\n\t"
      "s_branch L_pubb_%=\n\t"
      "L_pubvb_%=:\n\t"
      "s_nop 7\n\t"
      "v_add_f32 v136, v136, v138\n\t"
      "v_add_f32 v137, v137, v139\n\t"
      "v_add_f32 v140, v140, v142\n\t"
      "v_add_f32 v141, v141, v143\n\t"
      "v_cvt_pk_bf16_f32 v156, v136, v137\n\t"
      "v_cvt_pk_bf16_f32 v157, v140, v141\n\t"
      "L_pubb_%=:\n\t"
      "s_add_u32 s46, s46, 131072\n\t"
      "buffer_store_dwordx2 v[156:157], %[kvp], s[96:99], s46 offen sc1\n\t"
      "v_mov_b32 %[oe], 63\n\t"
      "s_waitcnt vmcnt(0)\n\t"
      "s_barrier\n\t"
      "buffer_store_dword %[oe], %[trq], s[96:99], s51 offen sc1\n\t"
      "s_waitcnt vmcnt(0) lgkmcnt(0)\n\t"
      "s_mov_b32 m0, s64\n\t"
      : [qkv] "+v"(qkv), [oe] "+v"(oe), [trq] "+v"(trq), [tro] "+v"(tro), [m0o] "+v"(m0o)
      : [idx] "v"(idx), [pst] "v"(pst), [pld] "v"(pld),
        [mst] "v"(mst), [mwr] "v"(mwr), [xwr] "v"(xwr), [xrd] "v"(xrd), [owr] "v"(owr), [kvp] "v"(kvp), [wvrd] "v"(wvrd), [memrd] "v"(memrd),
        [ones] "v"(ones), [onesm] "v"(onesm), [selb] "v"(selb),
        [wq] "s"(p.wq), [wk] "s"(p.wk), [wv] "s"(p.wv), [wo] "s"(p.wo), [mem0] "s"(p.mem0), [wsp] "s"(p.ws), [tag] "s"(tag64), [haw] "s"(haw)
      : "memory", "vcc", "scc",
        "v32", "v33", "v34", "v35", "v36", "v37", "v38", "v39", "v40", "v41", "v42", "v43", "v44", "v45", "v46", "v47", "v48", "v49", "v50", "v51", "v52",
        "v53", "v54", "v55", "v56", "v57", "v58", "v59", "v60", "v61", "v62", "v63", "v64", "v65", "v66", "v67", "v68", "v69", "v70", "v71", "v72", "v73",
        "v74", "v75", "v76", "v77", "v78", "v79", "v80", "v81", "v82", "v83", "v84", "v85", "v86", "v87", "v88", "v89", "v90", "v91", "v92", "v93", "v94",
        "v95", "v96", "v97", "v98", "v99", "v100", "v101", "v102", "v103", "v104", "v105", "v106", "v107", "v108", "v109", "v110", "v111", "v112", "v113",
        "v114", "v115", "v116", "v117", "v118", "v119", "v120", "v121", "v122", "v123", "v124", "v125", "v126", "v127", "v128", "v129", "v130", "v131",
        "v132", "v133", "v134", "v135", "v136", "v137", "v138", "v139", "v140", "v141", "v142", "v143", "v144", "v145", "v146", "v147", "v148", "v149",
        "v150", "v151", "v152", "v153", "v154", "v155", "v156", "v157", "v158", "v159", "v160", "v161", "v162", "v163", "v164", "v165", "v166", "v167",
        "v168", "v169", "v170", "v171", "v172", "v173", "v174", "v175", "v176", "v177", "v178", "v179", "v180", "v181", "v182", "v183", "v184", "v185",
        "v186", "v187", "v188", "v189", "v190", "v191", "v192", "v193", "v194", "v195", "v196", "v197", "v198", "v199", "v200", "v201", "v202", "v203",
        "v204", "v205", "v206", "v207", "v208", "v209", "v210", "v211", "v212", "v213", "v214", "v215", "v216", "v217", "v218", "v219", "v220", "v221",
        "v222", "v223", "v224", "v225", "v226", "v227", "v228", "v229", "v230", "v231", "v232", "v233", "v234", "v235", "v236", "v237", "v238", "v239",
        "v240", "v241", "v242", "v243", "v244", "v245", "v246", "v247", "v248", "v249", "v250", "v251", "v252", "v253", "v254", "v255", "a0", "a1", "a2",
        "a3", "a4", "a5", "a6", "a7", "a8", "a9", "a10", "a11", "a12", "a13", "a14", "a15", "a16", "a17", "a18", "a19", "a20", "a21", "a22", "a23", "a24",
        "a25", "a26", "a27", "a28", "a29", "a30", "a31", "a32", "a33", "a34", "a35", "a36", "a37", "a38", "a39", "a40", "a41", "a42", "a43", "a44", "a45",
        "a46", "a47", "a48", "a49", "a50", "a51", "a52", "a53", "a54", "a55", "a56", "a57", "a58", "a59", "a60", "a61", "a62", "a63", "a64", "a65", "a66",
        "a67", "a68", "a69", "a70", "a71", "a72", "a73", "a74", "a75", "a76", "a77", "a78", "a79", "a80", "a81", "a82", "a83", "a84", "a85", "a86", "a87",
        "a88", "a89", "a90", "a91", "a92", "a93", "a94", "a95", "a96", "a97", "a98", "a99", "a100", "a101", "a102", "a103", "a104", "a105", "a106", "a107",
        "a108", "a109", "a110", "a111", "a112", "a113", "a114", "a115", "a116", "a117", "a118", "a119", "a120", "a121", "a122", "a123", "a124", "a125",
        "a126", "a127", "a128", "a129", "a130", "a131", "a132", "a133", "a134", "a135", "a136", "a137", "a138", "a139", "a140", "a141", "a142", "a143",
        "a144", "a145", "a146", "a147", "a148", "a149", "a150", "a151", "a152", "a153", "a154", "a155", "a156", "a157", "a158", "a159", "a160", "a161",
        "a162", "a163", "a164", "a165", "a166", "a167", "a168", "a169", "a170", "a171", "a172", "a173", "a174", "a175", "a176", "a177", "a178", "a179",
        "a180", "a181", "a182", "a183", "a184", "a185", "a186", "a187", "a188", "a189", "a190", "a191", "a192", "a193", "a194", "a195", "a196", "a197",
        "a198", "a199", "a200", "a201", "a202", "a203", "a204", "a205", "a206", "a207", "a208", "a209", "a210", "a211", "a212", "a213", "a214", "a215",
        "a216", "a217", "a218", "a219", "a220", "a221", "a222", "a223", "a224", "a225", "a226", "a227", "a228", "a229", "a230", "a231", "a232", "a233",
        "a234", "a235", "a236", "a237", "a238", "a239", "a240", "a241", "a242", "a243", "a244", "a245", "a246", "a247", "a248", "a249", "a250", "a251",
        "a252", "a253", "a254", "a255", "s27", "s28", "s29", "s30", "s31", "s32", "s33", "s34", "s35", "s36", "s37", "s38", "s39", "s40", "s41", "s42", "s43", "s44", "s45", "s46", "s47", "s48", "s49", "s50", "s51", "s52", "s53", "s54", "s55", "s56",
        "s57", "s58", "s59", "s60", "s61", "s62", "s63", "s64", "s65", "s66", "s67", "s68", "s69", "s70", "s71", "s72", "s73", "s74", "s75", "s76", "s77",
        "s78", "s79", "s80", "s81", "s82", "s83", "s84", "s85", "s86", "s87", "s88", "s89", "s90", "s91", "s92", "s93", "s94", "s95", "s96", "s97", "s98", "s99"
      );
}

DI void stage128(const void* base, unsigned off0, unsigned row_stride, unsigned ldsbuf, int w, int lane) {
#pragma unroll
  for (int i = 0; i < 8; ++i) {
    const int r4 = lane >> 4;
    dma16(base, off0 + (unsigned)(32 * w + 4 * i + r4) * row_stride + (unsigned)(((lane & 15) ^ (r4 << 2)) * 16), (unsigned)rfl(ldsbuf + (32 * w + 4 * i) * 256));
  }
}

DI void warm_rows(const __bf16* W, lptr lds, int xq, int idx  ) {
  const int lane = threadIdx.x & 63, w = rfl(threadIdx.x >> 6);
  const unsigned lds0 = (unsigned)(size_t)lds, off = (unsigned)((idx * 2 + (w >> 1)) * 8192 + xq * 2048 + (w & 1) * 1024 + lane * 16);
#pragma unroll
  for (int i = 0; i < 16; ++i) dma16(W, off + i * 32 * 8192, (unsigned)rfl(lds0 + 98304 + w * 8192 + (i & 7) * 1024));
}
DI void warm_linear(const __bf16* W, lptr lds, int xq, int idx) {
  const int lane = threadIdx.x & 63, w = rfl(threadIdx.x >> 6);
  const unsigned lds0 = (unsigned)(size_t)lds, off = (unsigned)(xq * 1048576 + idx * 65536 + w * 16384 + lane * 16);
#pragma unroll
  for (int i = 0; i < 16; ++i) dma16(W, off + i * 1024, (unsigned)rfl(lds0 + 98304 + w * 8192 + (i & 7) * 1024));
}

constexpr int X_XS = 0, X_Q = 65536;
typedef unsigned u32x32 __attribute__((ext_vector_type(32)));
#define XW_CLOB_S "memory", "vcc", "scc", "s40", "s41", "s42", "s44", "s45"
#define KV_CLOB "v32", "v33", "v34", "v35", "v36", "v37", "v38", "v39", "v40", "v41", "v42", "v43", "v44", "v45", "v46", "v47", "v48", "v49", "v50", "v51", "v52",\
        "v53", "v54", "v55", "v56", "v57", "v58", "v59", "v60", "v61", "v62", "v63", "v64", "v65", "v66", "v67", "v68", "v69", "v70", "v71", "v72", "v73",\
        "v74", "v75", "v76", "v77", "v78", "v79", "v80", "v81", "v82", "v83", "v84", "v85", "v86", "v87", "v88", "v89", "v90", "v91", "v92", "v93", "v94",\
        "v95", "v96", "v97", "v98", "v99", "v100", "v101", "v102", "v103", "v104", "v105", "v106", "v107", "v108", "v109", "v110", "v111"
#define QA_CLOB "v32", "v33", "v34", "v35", "v36", "v37", "v38", "v39", "v40", "v41", "v42", "v43", "v44", "v45", "v46", "v47", "v48", "v49", "v50", "v51", "v52",\
        "v53", "v54", "v55", "v56", "v57", "v58", "v59", "v60", "v61", "v62", "v63", "v64", "v65", "v66", "v67", "v68", "v69", "v70", "v71", "v72", "v73",\
        "v74", "v75", "v76", "v77", "v78", "v79", "v80", "v81", "v82", "v83", "v84", "v85", "v86", "v87", "v88", "v89", "v90", "v91", "v92", "v93", "v94",\
        "v95", "v96", "v97", "v98", "v99", "v100", "v101", "v102", "v103", "v104", "v105", "v106", "v107", "v108", "v109", "v110", "v111", "v112", "v113",\
        "v114", "v115", "v116", "v117", "v118", "v119", "v120", "v121", "v122", "v123", "v124", "v125", "v126", "v127", "v128", "v129", "v130", "v131",\
        "v132", "v133", "v134", "v135", "v136", "v137", "v138", "v139", "v140", "v141", "v142", "v143", "v144", "v145", "v146", "v147", "v148", "v149",\
        "v150", "v151", "v152", "v153", "v154", "v155", "v156", "v157", "v158", "v159", "v160", "v161", "v162", "v163", "v164", "v165", "v166", "v167",\
        "v168", "v169", "v170", "v171", "v172", "v173", "v174", "v175", "v176", "v177", "v178", "v179", "v180", "v181", "v182", "v183", "v184", "v185",\
        "v186", "v187", "v188", "v189", "v190", "v191", "v192", "v193", "v194", "v195", "v196", "v197", "v198", "v199", "v200", "v201", "v202", "v203",\
        "v204", "v205", "v206", "v207", "v208", "v209", "v210", "v211", "v212", "v213", "v214", "v215", "v216", "v217", "v218", "v219", "v220", "v221",\
        "v222", "v223", "v224", "v225", "v226", "v227", "v228", "v229", "v230", "v231"

struct XwCommon {
  const Params& p; lptr lds; int h, w;
  DI void dma_x(int s, int buf) const {
    const unsigned l16 = (unsigned)((fresh_lane() ^ (8 * (w & 1))) * 16), lds0 = (unsigned)(size_t)lds;
#pragma unroll
    for (int i = 0; i < 8; ++i) dma16(p.ax + (size_t)(32 * s + 8 * w + i) * 512, l16 ^ (unsigned)(i * 16), (unsigned)rfl(lds0 + X_XS + buf * 32768 + (8 * w + i) * 1024));
  }
  DI void stage(const __bf16* W, int cc, int buf) const { stage128(W, (unsigned)((128 * cc * 4096 + h * 128) * 2), 8192u, (unsigned)(size_t)lds + 32768 + buf * 32768, w, fresh_lane()); }
  DI u32x32 unpack(int buf) const {
    const int lane = fresh_lane(), hh = lane >> 5, q4 = (lane & 15) >> 2, pp = lane & 3;
    lptr b = lds + 32768 + buf * 32768 + (8 * hh + q4) * 256 + ((4 * w + 2 * ((lane >> 4) & 1) + (pp >> 1)) ^ (q4 << 2)) * 16 + 8 * (pp & 1);
    u32x32 r;
#pragma unroll
    for (int k = 0; k < 8; ++k) { const u32x4 f = __builtin_bit_cast(u32x4, tr2(b + k * 4096, b + k * 4096 + 1024)); r[4 * k] = f[0]; r[4 * k + 1] = f[1]; r[4 * k + 2] = f[2]; r[4 * k + 3] = f[3]; }
    return r;
  }
};

DI void kv_worker(const Params& p, rsrc_t ws, lptr lds, int h, Tag tag) {
  const int w = rfl(threadIdx.x >> 6);
  const XwCommon c{p, lds, h, w};
  const unsigned lds0 = (unsigned)(size_t)lds;
  const size_t wsa = (size_t)p.ws;
  const u32x4 srd = {(unsigned)wsa, (unsigned)(wsa >> 32) & 0xffffu, WS_BYTES, 0x00020000u};
  u32x32 W[8];
  c.dma_x(0, 0);
  c.stage(p.wk, 0, 0); c.stage(p.wk, 1, 1); c.stage(p.wk, 2, 2);
#pragma unroll
  for (int i = 0; i < 8; ++i) {
    if (i < 6) VM_LDS_BARRIER(16); else if (i == 6) VM_LDS_BARRIER(8); else VM_LDS_BARRIER(0);
    if (i + 3 < 8) c.stage(i + 3 < 4 ? p.wk : p.wv, (i + 3) & 3, (i + 3) & 3);
    W[i] = c.unpack(i & 3);
  }
  LDS_BARRIER();
  const unsigned kvf = F_KVF + (unsigned)(h * 64 + (wait_tag(ws, F_SEL, tag) & 1) * 8192);
#pragma unroll 1
  for (int s = 0; s < 64; ++s) {
    const int lane = fresh_lane(), r32 = lane & 31, hh = lane >> 5, buf = s & 1;
    const unsigned xa = lds0 + X_XS + buf * 32768 + r32 * 1024 + ((hh ^ (r32 & 15)) * 16), l16 = (unsigned)((lane ^ (8 * (w & 1))) * 16);
    const unsigned lo = (unsigned)((16 * hh + (r32 & 15)) * 16), gk = lo + (unsigned)(((r32 >> 4) * 4 + w) * 1024), gv = lo + (unsigned)((2 * w + (r32 >> 4)) * 1024);
    const unsigned skv = KV_OFF + (unsigned)(s * 32 + h) * 16384, m0n = lds0 + X_XS + (buf ^ 1) * 32768 + w * 8192;
    const __bf16* xn = p.ax + (size_t)(32 * (s < 63 ? s + 1 : s) + 8 * w) * 512;
    asm volatile(
      "s_waitcnt lgkmcnt(0)\n\t"
      "s_mov_b32 s42, m0\n\t"
      "s_mov_b64 s[44:45], %[xn]\n\t"
      "v_mov_b32 v32, %[xa]\n\t"
      "v_xor_b32 v33, 32, %[xa]\n\t"
      "v_xor_b32 v34, 64, %[xa]\n\t"
      "v_xor_b32 v35, 96, %[xa]\n\t"
      "v_xor_b32 v36, 128, %[xa]\n\t"
      "v_xor_b32 v37, 160, %[xa]\n\t"
      "v_xor_b32 v38, 192, %[xa]\n\t"
      "v_xor_b32 v39, 224, %[xa]\n\t"
      "ds_read_b128 v[40:43], v32 offset:0\n\t"
      "ds_read_b128 v[44:47], v33 offset:0\n\t"
      "ds_read_b128 v[48:51], v34 offset:0\n\t"
      "s_waitcnt lgkmcnt(2)\n\t"
      "v_mfma_f32_32x32x16_bf16 v[64:79], a[0:3], v[40:43], 0\n\t"
      "v_mfma_f32_32x32x16_bf16 v[80:95], v[40:43], a[128:131], 0\n\t"
      "ds_read_b128 v[52:55], v35 offset:0\n\t"
      "s_waitcnt lgkmcnt(2)\n\t"
      "v_mfma_f32_32x32x16_bf16 v[64:79], a[4:7], v[44:47], v[64:79]\n\t"
      "s_add_u32 m0, %[m0n], 0\n\t"
      "v_xor_b32 v56, 0, %[l16]\n\t"
      "global_load_lds_dwordx4 v56, s[44:45]\n\t"
      "s_add_u32 s44, s44, 1024\n\t"
      "s_addc_u32 s45, s45, 0\n\t"
      "v_mfma_f32_32x32x16_bf16 v[80:95], v[44:47], a[132:135], v[80:95]\n\t"
      "ds_read_b128 v[40:43], v36 offset:0\n\t"
      "s_waitcnt lgkmcnt(2)\n\t"
      "v_mfma_f32_32x32x16_bf16 v[64:79], a[8:11], v[48:51], v[64:79]\n\t"
      "v_mfma_f32_32x32x16_bf16 v[80:95], v[48:51], a[136:139], v[80:95]\n\t"
      "ds_read_b128 v[44:47], v37 offset:0\n\t"
      "s_waitcnt lgkmcnt(2)\n\t"
      "v_mfma_f32_32x32x16_bf16 v[64:79], a[12:15], v[52:55], v[64:79]\n\t"
      "s_add_u32 m0, %[m0n], 1024\n\t"
      "v_xor_b32 v56, 16, %[l16]\n\t"
      "global_load_lds_dwordx4 v56, s[44:45]\n\t"
      "s_add_u32 s44, s44, 1024\n\t"
      "s_addc_u32 s45, s45, 0\n\t"
      "v_mfma_f32_32x32x16_bf16 v[80:95], v[52:55], a[140:143], v[80:95]\n\t"
      "ds_read_b128 v[48:51], v38 offset:0\n\t"
      "s_waitcnt lgkmcnt(2)\n\t"
      "v_mfma_f32_32x32x16_bf16 v[64:79], a[16:19], v[40:43], v[64:79]\n\t"
      "v_mfma_f32_32x32x16_bf16 v[80:95], v[40:43], a[144:147], v[80:95]\n\t"
      "ds_read_b128 v[52:55], v39 offset:0\n\t"
      "s_waitcnt lgkmcnt(2)\n\t"
      "v_mfma_f32_32x32x16_bf16 v[64:79], a[20:23], v[44:47], v[64:79]\n\t"
      "s_add_u32 m0, %[m0n], 2048\n\t"
      "v_xor_b32 v56, 32, %[l16]\n\t"
      "global_load_lds_dwordx4 v56, s[44:45]\n\t"
      "s_add_u32 s44, s44, 1024\n\t"
      "s_addc_u32 s45, s45, 0\n\t"
      "v_mfma_f32_32x32x16_bf16 v[80:95], v[44:47], a[148:151], v[80:95]\n\t"
      "ds_read_b128 v[40:43], v32 offset:256\n\t"
      "s_waitcnt lgkmcnt(2)\n\t"
      "v_mfma_f32_32x32x16_bf16 v[64:79], a[24:27], v[48:51], v[64:79]\n\t"
      "v_mfma_f32_32x32x16_bf16 v[80:95], v[48:51], a[152:155], v[80:95]\n\t"
      "ds_read_b128 v[44:47], v33 offset:256\n\t"
      "s_waitcnt lgkmcnt(2)\n\t"
      "v_mfma_f32_32x32x16_bf16 v[64:79], a[28:31], v[52:55], v[64:79]\n\t"
      "s_add_u32 m0, %[m0n], 3072\n\t"
      "v_xor_b32 v56, 48, %[l16]\n\t"
      "global_load_lds_dwordx4 v56, s[44:45]\n\t"
      "s_add_u32 s44, s44, 1024\n\t"
      "s_addc_u32 s45, s45, 0\n\t"
      "v_mfma_f32_32x32x16_bf16 v[80:95], v[52:55], a[156:159], v[80:95]\n\t"
      "ds_read_b128 v[48:51], v34 offset:256\n\t"
      "s_waitcnt lgkmcnt(2)\n\t"
      "v_mfma_f32_32x32x16_bf16 v[64:79], a[32:35], v[40:43], v[64:79]\n\t"
      "v_mfma_f32_32x32x16_bf16 v[80:95], v[40:43], a[160:163], v[80:95]\n\t"
      "ds_read_b128 v[52:55], v35 offset:256\n\t"
      "s_waitcnt lgkmcnt(2)\n\t"
      "v_mfma_f32_32x32x16_bf16 v[64:79], a[36:39], v[44:47], v[64:79]\n\t"
      "s_add_u32 m0, %[m0n], 4096\n\t"
      "v_xor_b32 v56, 64, %[l16]\n\t"
      "global_load_lds_dwordx4 v56, s[44:45]\n\t"
      "s_add_u32 s44, s44, 1024\n\t"
      "s_addc_u32 s45, s45, 0\n\t"
      "v_mfma_f32_32x32x16_bf16 v[80:95], v[44:47], a[164:167], v[80:95]\n\t"
      "ds_read_b128 v[40:43], v36 offset:256\n\t"
      "s_waitcnt lgkmcnt(2)\n\t"
      "v_mfma_f32_32x32x16_bf16 v[64:79], a[40:43], v[48:51], v[64:79]\n\t"
      "v_mfma_f32_32x32x16_bf16 v[80:95], v[48:51], a[168:171], v[80:95]\n\t"
      "ds_read_b128 v[44:47], v37 offset:256\n\t"
      "s_waitcnt lgkmcnt(2)\n\t"
      "v_mfma_f32_32x32x16_bf16 v[64:79], a[44:47], v[52:55], v[64:79]\n\t"
      "s_add_u32 m0, %[m0n], 5120\n\t"
      "v_xor_b32 v56, 80, %[l16]\n\t"
      "global_load_lds_dwordx4 v56, s[44:45]\n\t"
      "s_add_u32 s44, s44, 1024\n\t"
      "s_addc_u32 s45, s45, 0\n\t"
      "v_mfma_f32_32x32x16_bf16 v[80:95], v[52:55], a[172:175], v[80:95]\n\t"
      "ds_read_b128 v[48:51], v38 offset:256\n\t"
      "s_waitcnt lgkmcnt(2)\n\t"
      "v_mfma_f32_32x32x16_bf16 v[64:79], a[48:51], v[40:43], v[64:79]\n\t"
      "v_mfma_f32_32x32x16_bf16 v[80:95], v[40:43], a[176:179], v[80:95]\n\t"
      "ds_read_b128 v[52:55], v39 offset:256\n\t"
      "s_waitcnt lgkmcnt(2)\n\t"
      "v_mfma_f32_32x32x16_bf16 v[64:79], a[52:55], v[44:47], v[64:79]\n\t"
      "s_add_u32 m0, %[m0n], 6144\n\t"
      "v_xor_b32 v56, 96, %[l16]\n\t"
      "global_load_lds_dwordx4 v56, s[44:45]\n\t"
      "s_add_u32 s44, s44, 1024\n\t"
      "s_addc_u32 s45, s45, 0\n\t"
      "v_mfma_f32_32x32x16_bf16 v[80:95], v[44:47], a[180:183], v[80:95]\n\t"
      "ds_read_b128 v[40:43], v32 offset:512\n\t"
      "s_waitcnt lgkmcnt(2)\n\t"
      "v_mfma_f32_32x32x16_bf16 v[64:79], a[56:59], v[48:51], v[64:79]\n\t"
      "v_mfma_f32_32x32x16_bf16 v[80:95], v[48:51], a[184:187], v[80:95]\n\t"
      "ds_read_b128 v[44:47], v33 offset:512\n\t"
      "s_waitcnt lgkmcnt(2)\n\t"
      "v_mfma_f32_32x32x16_bf16 v[64:79], a[60:63], v[52:55], v[64:79]\n\t"
      "s_add_u32 m0, %[m0n], 7168\n\t"
      "v_xor_b32 v56, 112, %[l16]\n\t"
      "global_load_lds_dwordx4 v56, s[44:45]\n\t"
      "s_add_u32 s44, s44, 1024\n\t"
      "s_addc_u32 s45, s45, 0\n\t"
      "v_mfma_f32_32x32x16_bf16 v[80:95], v[52:55], a[188:191], v[80:95]\n\t"
      "ds_read_b128 v[48:51], v34 offset:512\n\t"
      "s_waitcnt lgkmcnt(2)\n\t"
      "v_mfma_f32_32x32x16_bf16 v[64:79], a[64:67], v[40:43], v[64:79]\n\t"
      "v_mfma_f32_32x32x16_bf16 v[80:95], v[40:43], a[192:195], v[80:95]\n\t"
      "ds_read_b128 v[52:55], v35 offset:512\n\t"
      "s_waitcnt lgkmcnt(2)\n\t"
      "v_mfma_f32_32x32x16_bf16 v[64:79], a[68:71], v[44:47], v[64:79]\n\t"
      "v_mfma_f32_32x32x16_bf16 v[80:95], v[44:47], a[196:199], v[80:95]\n\t"
      "ds_read_b128 v[40:43], v36 offset:512\n\t"
      "s_waitcnt lgkmcnt(2)\n\t"
      "v_mfma_f32_32x32x16_bf16 v[64:79], a[72:75], v[48:51], v[64:79]\n\t"
      "v_mfma_f32_32x32x16_bf16 v[80:95], v[48:51], a[200:203], v[80:95]\n\t"
      "ds_read_b128 v[44:47], v37 offset:512\n\t"
      "s_waitcnt lgkmcnt(2)\n\t"
      "v_mfma_f32_32x32x16_bf16 v[64:79], a[76:79], v[52:55], v[64:79]\n\t"
      "v_mfma_f32_32x32x16_bf16 v[80:95], v[52:55], a[204:207], v[80:95]\n\t"
      "ds_read_b128 v[48:51], v38 offset:512\n\t"
      "s_waitcnt lgkmcnt(2)\n\t"
      "v_mfma_f32_32x32x16_bf16 v[64:79], a[80:83], v[40:43], v[64:79]\n\t"
      "v_mfma_f32_32x32x16_bf16 v[80:95], v[40:43], a[208:211], v[80:95]\n\t"
      "ds_read_b128 v[52:55], v39 offset:512\n\t"
      "s_waitcnt lgkmcnt(2)\n\t"
      "v_mfma_f32_32x32x16_bf16 v[64:79], a[84:87], v[44:47], v[64:79]\n\t"
      "v_mfma_f32_32x32x16_bf16 v[80:95], v[44:47], a[212:215], v[80:95]\n\t"
      "ds_read_b128 v[40:43], v32 offset:768\n\t"
      "s_waitcnt lgkmcnt(2)\n\t"
      "v_mfma_f32_32x32x16_bf16 v[64:79], a[88:91], v[48:51], v[64:79]\n\t"
      "v_mfma_f32_32x32x16_bf16 v[80:95], v[48:51], a[216:219], v[80:95]\n\t"
      "ds_read_b128 v[44:47], v33 offset:768\n\t"
      "s_waitcnt lgkmcnt(2)\n\t"
      "v_mfma_f32_32x32x16_bf16 v[64:79], a[92:95], v[52:55], v[64:79]\n\t"
      "v_mfma_f32_32x32x16_bf16 v[80:95], v[52:55], a[220:223], v[80:95]\n\t"
      "ds_read_b128 v[48:51], v34 offset:768\n\t"
      "s_waitcnt lgkmcnt(2)\n\t"
      "v_mfma_f32_32x32x16_bf16 v[64:79], a[96:99], v[40:43], v[64:79]\n\t"
      "v_mfma_f32_32x32x16_bf16 v[80:95], v[40:43], a[224:227], v[80:95]\n\t"
      "ds_read_b128 v[52:55], v35 offset:768\n\t"
      "s_waitcnt lgkmcnt(2)\n\t"
      "v_mfma_f32_32x32x16_bf16 v[64:79], a[100:103], v[44:47], v[64:79]\n\t"
      "v_mfma_f32_32x32x16_bf16 v[80:95], v[44:47], a[228:231], v[80:95]\n\t"
      "ds_read_b128 v[40:43], v36 offset:768\n\t"
      "s_waitcnt lgkmcnt(2)\n\t"
      "v_mfma_f32_32x32x16_bf16 v[64:79], a[104:107], v[48:51], v[64:79]\n\t"
      "v_mfma_f32_32x32x16_bf16 v[80:95], v[48:51], a[232:235], v[80:95]\n\t"
      "ds_read_b128 v[44:47], v37 offset:768\n\t"
      "s_waitcnt lgkmcnt(2)\n\t"
      "v_mfma_f32_32x32x16_bf16 v[64:79], a[108:111], v[52:55], v[64:79]\n\t"
      "v_mfma_f32_32x32x16_bf16 v[80:95], v[52:55], a[236:239], v[80:95]\n\t"
      "ds_read_b128 v[48:51], v38 offset:768\n\t"
      "s_waitcnt lgkmcnt(2)\n\t"
      "v_mfma_f32_32x32x16_bf16 v[64:79], a[112:115], v[40:43], v[64:79]\n\t"
      "v_mfma_f32_32x32x16_bf16 v[80:95], v[40:43], a[240:243], v[80:95]\n\t"
      "ds_read_b128 v[52:55], v39 offset:768\n\t"
      "s_waitcnt lgkmcnt(2)\n\t"
      "v_mfma_f32_32x32x16_bf16 v[64:79], a[116:119], v[44:47], v[64:79]\n\t"
      "v_mfma_f32_32x32x16_bf16 v[80:95], v[44:47], a[244:247], v[80:95]\n\t"
      "s_waitcnt lgkmcnt(1)\n\t"
      "v_mfma_f32_32x32x16_bf16 v[64:79], a[120:123], v[48:51], v[64:79]\n\t"
      "v_mfma_f32_32x32x16_bf16 v[80:95], v[48:51], a[248:251], v[80:95]\n\t"
      "s_waitcnt lgkmcnt(0)\n\t"
      "v_mfma_f32_32x32x16_bf16 v[64:79], a[124:127], v[52:55], v[64:79]\n\t"
      "v_mfma_f32_32x32x16_bf16 v[80:95], v[52:55], a[252:255], v[80:95]\n\t"
      "s_add_u32 s40, %[skv], 8192\n\t"
      "s_mov_b32 m0, s42\n\t"
      "s_nop 8\n\t"
      "v_cvt_pk_bf16_f32 v96, v64, v65\n\t"
      "v_cvt_pk_bf16_f32 v97, v66, v67\n\t"
      "v_cvt_pk_bf16_f32 v98, v72, v73\n\t"
      "v_cvt_pk_bf16_f32 v99, v74, v75\n\t"
      "buffer_store_dwordx4 v[96:99], %[gk], %[srd], %[skv] offen offset:0 sc1\n\t"
      "v_cvt_pk_bf16_f32 v100, v68, v69\n\t"
      "v_cvt_pk_bf16_f32 v101, v70, v71\n\t"
      "v_cvt_pk_bf16_f32 v102, v76, v77\n\t"
      "v_cvt_pk_bf16_f32 v103, v78, v79\n\t"
      "buffer_store_dwordx4 v[100:103], %[gk], %[srd], %[skv] offen offset:512 sc1\n\t"
      "v_cvt_pk_bf16_f32 v104, v80, v81\n\t"
      "v_cvt_pk_bf16_f32 v105, v82, v83\n\t"
      "v_cvt_pk_bf16_f32 v106, v88, v89\n\t"
      "v_cvt_pk_bf16_f32 v107, v90, v91\n\t"
      "buffer_store_dwordx4 v[104:107], %[gv], %[srd], s40 offen offset:0 sc1\n\t"
      "v_cvt_pk_bf16_f32 v108, v84, v85\n\t"
      "v_cvt_pk_bf16_f32 v109, v86, v87\n\t"
      "v_cvt_pk_bf16_f32 v110, v92, v93\n\t"
      "v_cvt_pk_bf16_f32 v111, v94, v95\n\t"
      "buffer_store_dwordx4 v[108:111], %[gv], %[srd], s40 offen offset:512 sc1\n\t"
      "s_nop 1\n\t"
      :: [xa] "v"(xa), [gk] "v"(gk), [gv] "v"(gv), [l16] "v"(l16), [srd] "s"(srd), [skv] "s"(skv), [xn] "s"(xn), [m0n] "s"(m0n),
         "{a[0:31]}"(W[0]), "{a[32:63]}"(W[1]), "{a[64:95]}"(W[2]), "{a[96:127]}"(W[3]), "{a[128:159]}"(W[4]), "{a[160:191]}"(W[5]), "{a[192:223]}"(W[6]), "{a[224:255]}"(W[7])
      : XW_CLOB_S, KV_CLOB);
    const bool now = s < 8 || s == 63;
    if (now) VM_LDS_BARRIER(0); else VM_LDS_BARRIER(4);
    if (w == 0 && fresh_lane() == 0) gst4(ws, kvf, (unsigned)(now ? s : s - 1));
  }
}

#define QA_SPLIT 44
DI void qa_worker(const Params& p, rsrc_t ws, lptr lds, int h, int first, Tag tag) {
  const int w = rfl(threadIdx.x >> 6);
  const XwCommon c{p, lds, h, w};
  const unsigned lds0 = (unsigned)(size_t)lds;
  const size_t wsa = (size_t)p.ws;
  const u32x4 srd = {(unsigned)wsa, (unsigned)(wsa >> 32) & 0xffffu, WS_BYTES, 0x00020000u};
  u32x32 W[4];
  if (first == 0) for (int i = 0; i < 5; ++i) __builtin_amdgcn_s_sleep(32);
  c.dma_x(first, 0);
#pragma unroll
  for (int i = 0; i < 4; ++i) c.stage(p.wq, i, i);
  VM_LDS_BARRIER(0);
#pragma unroll
  for (int i = 0; i < 4; ++i) W[i] = c.unpack(i);
  LDS_BARRIER();
  {
    const int lane = fresh_lane();
    f32x4 mk;
#pragma unroll
    for (int r = 0; r < 4; ++r) mk[r] = crow(4 * w + r, lane >> 5) > (lane & 31) ? -INFINITY : 0.f;
    *(LAS f32x4*)(lds + X_Q + 8192 + w * 1024 + lane * 16) = mk;
  }
  const unsigned sel13 = (unsigned)(wait_tag(ws, F_SEL, tag) & 1) * 8192;
  int avail = -1, spin = 0, prev = -1, buf = 0, early = -1;
#pragma unroll 1
  for (int s = first; s < 64;) {
    const int nxt = s < QA_SPLIT ? s + 1 : s + 2;
    if (avail < s) avail = wait_ge(ws, F_KVF + sel13 + (unsigned)h * 64, s, 8);
    VM_LDS_BARRIER(2);
    const int lane = fresh_lane(), r32 = lane & 31, hh = lane >> 5;
    const unsigned xa = lds0 + X_XS + buf * 32768 + r32 * 1024 + ((hh ^ (r32 & 15)) * 16), l16 = (unsigned)((lane ^ (8 * (w & 1))) * 16);
    const unsigned lo = (unsigned)((16 * hh + (r32 & 15)) * 16), gv = lo + (unsigned)((2 * w + (r32 >> 4)) * 1024);
    const unsigned qr = lds0 + X_Q + lane * 16, qw = qr + w * 2048, kr = lds0 + X_Q + lo + (r32 >> 4) * 4096, mk = lds0 + X_Q + w * 2048, mq = lds0 + X_Q + w * 512;
    const unsigned kmr = lds0 + X_Q + (hh * 8 + (r32 & 7)) * 16, gq = (unsigned)(w * 512 + lane * 16), gvm = (unsigned)(((16 * w + (r32 & 15)) * 4 + 2 * hh) * 8 + (r32 >> 4) * 4), gox = (unsigned)(w * 2048 + lane * 16);
    const unsigned gf = (unsigned)((lane & 1) * 64), gof = (w == 0 && lane == 0) ? 0u : 0x80000000u, one = (unsigned)prev;
    const unsigned skv = KV_OFF + (unsigned)(s * 32 + h) * 16384, m0n = lds0 + X_XS + (buf ^ 1) * 32768 + w * 8192;
    const unsigned skm = KM_OFF + (unsigned)(s * 32 + h) * 4096, sox = OX_OFF + (unsigned)(s * 32 + h) * 8192, skf = F_KMF + sel13 + (unsigned)h * 128;
    const unsigned sof = F_OXF + (unsigned)(h * 128 + (first ? 64 : 0));
    const __bf16* xn = p.ax + (size_t)(32 * (nxt < 64 ? nxt : s) + 8 * w) * 512;
    asm volatile(
      "s_mov_b32 s41, 0x3e0293ee\n\t"
      "s_add_u32 s40, %[skv], 8192\n\t"
      "s_waitcnt lgkmcnt(0)\n\t"
      "s_mov_b32 s42, m0\n\t"
      "s_mov_b64 s[44:45], %[xn]\n\t"
      "v_mov_b32 v32, %[xa]\n\t"
      "v_xor_b32 v33, 32, %[xa]\n\t"
      "v_xor_b32 v34, 64, %[xa]\n\t"
      "v_xor_b32 v35, 96, %[xa]\n\t"
      "v_xor_b32 v36, 128, %[xa]\n\t"
      "v_xor_b32 v37, 160, %[xa]\n\t"
      "v_xor_b32 v38, 192, %[xa]\n\t"
      "v_xor_b32 v39, 224, %[xa]\n\t"
      "ds_read_b128 v[40:43], v32 offset:0\n\t"
      "ds_read_b128 v[44:47], v33 offset:0\n\t"
      "ds_read_b128 v[48:51], v34 offset:0\n\t"
      "s_waitcnt lgkmcnt(2)\n\t"
      "v_mfma_f32_32x32x16_bf16 v[64:79], a[0:3], v[40:43], 0\n\t"
      "ds_read_b128 v[52:55], v35 offset:0\n\t"
      "buffer_load_dword v57, %[gf], %[srd], %[skf] offen sc1\n\t"
      "s_waitcnt lgkmcnt(2)\n\t"
      "v_mfma_f32_32x32x16_bf16 v[64:79], a[4:7], v[44:47], v[64:79]\n\t"
      "ds_read_b128 v[40:43], v36 offset:0\n\t"
      "s_add_u32 m0, %[mk], 12288\n\t"
      "s_nop 0\n\t"
      "buffer_load_dwordx4 %[gox], %[srd], %[skv] offen sc1 lds\n\t"
      "s_waitcnt lgkmcnt(2)\n\t"
      "v_mfma_f32_32x32x16_bf16 v[64:79], a[8:11], v[48:51], v[64:79]\n\t"
      "ds_read_b128 v[44:47], v37 offset:0\n\t"
      "s_waitcnt lgkmcnt(2)\n\t"
      "v_mfma_f32_32x32x16_bf16 v[64:79], a[12:15], v[52:55], v[64:79]\n\t"
      "ds_read_b128 v[48:51], v38 offset:0\n\t"
      "buffer_load_dwordx4 %[gox], %[srd], %[skv] offen offset:1024 sc1 lds\n\t"
      "s_waitcnt lgkmcnt(2)\n\t"
      "v_mfma_f32_32x32x16_bf16 v[64:79], a[16:19], v[40:43], v[64:79]\n\t"
      "ds_read_b128 v[52:55], v39 offset:0\n\t"
      "s_waitcnt lgkmcnt(2)\n\t"
      "v_mfma_f32_32x32x16_bf16 v[64:79], a[20:23], v[44:47], v[64:79]\n\t"
      "ds_read_b128 v[40:43], v32 offset:256\n\t"
      "buffer_load_dwordx4 v[144:147], %[gv], %[srd], s40 offen sc1\n\t"
      "s_waitcnt lgkmcnt(2)\n\t"
      "v_mfma_f32_32x32x16_bf16 v[64:79], a[24:27], v[48:51], v[64:79]\n\t"
      "ds_read_b128 v[44:47], v33 offset:256\n\t"
      "s_waitcnt lgkmcnt(2)\n\t"
      "v_mfma_f32_32x32x16_bf16 v[64:79], a[28:31], v[52:55], v[64:79]\n\t"
      "ds_read_b128 v[48:51], v34 offset:256\n\t"
      "buffer_load_dwordx4 v[148:151], %[gv], %[srd], s40 offen offset:512 sc1\n\t"
      "s_waitcnt lgkmcnt(2)\n\t"
      "v_mfma_f32_32x32x16_bf16 v[64:79], a[32:35], v[40:43], v[64:79]\n\t"
      "ds_read_b128 v[52:55], v35 offset:256\n\t"
      "s_waitcnt lgkmcnt(2)\n\t"
      "v_mfma_f32_32x32x16_bf16 v[64:79], a[36:39], v[44:47], v[64:79]\n\t"
      "ds_read_b128 v[40:43], v36 offset:256\n\t"
      "s_cmp_lt_i32 %[early], %[seg]\n\t"
      "s_cbranch_scc1 L_late_%=\n\t"
      "s_add_u32 m0, %[mq], 20480\n\t"
      "s_mov_b32 exec_hi, 0\n\t"
      "buffer_load_dwordx4 %[gq], %[srd], %[skm] offen sc1 lds\n\t"
      "s_mov_b32 exec_hi, -1\n\t"
      "buffer_load_dword v184, %[gvm], %[srd], %[skm] offen offset:2048 sc1\n\t"
      "buffer_load_dword v185, %[gvm], %[srd], %[skm] offen offset:2056 sc1\n\t"
      "L_late_%=:\n\t"
      "s_waitcnt lgkmcnt(2)\n\t"
      "v_mfma_f32_32x32x16_bf16 v[64:79], a[40:43], v[48:51], v[64:79]\n\t"
      "ds_read_b128 v[44:47], v37 offset:256\n\t"
      "s_waitcnt lgkmcnt(2)\n\t"
      "v_mfma_f32_32x32x16_bf16 v[64:79], a[44:47], v[52:55], v[64:79]\n\t"
      "ds_read_b128 v[48:51], v38 offset:256\n\t"
      "s_waitcnt lgkmcnt(2)\n\t"
      "v_mfma_f32_32x32x16_bf16 v[64:79], a[48:51], v[40:43], v[64:79]\n\t"
      "ds_read_b128 v[52:55], v39 offset:256\n\t"
      "s_add_u32 m0, %[m0n], 0\n\t"
      "v_xor_b32 v56, 0, %[l16]\n\t"
      "global_load_lds_dwordx4 v56, s[44:45]\n\t"
      "s_add_u32 s44, s44, 1024\n\t"
      "s_addc_u32 s45, s45, 0\n\t"
      "s_waitcnt lgkmcnt(2)\n\t"
      "v_mfma_f32_32x32x16_bf16 v[64:79], a[52:55], v[44:47], v[64:79]\n\t"
      "ds_read_b128 v[40:43], v32 offset:512\n\t"
      "ds_read_b128 v[80:83], %[qr] offset:8192\n\t"
      "s_waitcnt lgkmcnt(3)\n\t"
      "v_mfma_f32_32x32x16_bf16 v[64:79], a[56:59], v[48:51], v[64:79]\n\t"
      "ds_read_b128 v[44:47], v33 offset:512\n\t"
      "s_add_u32 m0, %[m0n], 1024\n\t"
      "v_xor_b32 v56, 16, %[l16]\n\t"
      "global_load_lds_dwordx4 v56, s[44:45]\n\t"
      "s_add_u32 s44, s44, 1024\n\t"
      "s_addc_u32 s45, s45, 0\n\t"
      "s_waitcnt lgkmcnt(3)\n\t"
      "v_mfma_f32_32x32x16_bf16 v[64:79], a[60:63], v[52:55], v[64:79]\n\t"
      "ds_read_b128 v[48:51], v34 offset:512\n\t"
      "ds_read_b128 v[84:87], %[qr] offset:9216\n\t"
      "s_waitcnt lgkmcnt(4)\n\t"
      "v_mfma_f32_32x32x16_bf16 v[64:79], a[64:67], v[40:43], v[64:79]\n\t"
      "ds_read_b128 v[52:55], v35 offset:512\n\t"
      "s_add_u32 m0, %[m0n], 2048\n\t"
      "v_xor_b32 v56, 32, %[l16]\n\t"
      "global_load_lds_dwordx4 v56, s[44:45]\n\t"
      "s_add_u32 s44, s44, 1024\n\t"
      "s_addc_u32 s45, s45, 0\n\t"
      "s_waitcnt lgkmcnt(3)\n\t"
      "v_mfma_f32_32x32x16_bf16 v[64:79], a[68:71], v[44:47], v[64:79]\n\t"
      "ds_read_b128 v[40:43], v36 offset:512\n\t"
      "ds_read_b128 v[88:91], %[qr] offset:10240\n\t"
      "s_waitcnt lgkmcnt(4)\n\t"
      "v_mfma_f32_32x32x16_bf16 v[64:79], a[72:75], v[48:51], v[64:79]\n\t"
      "ds_read_b128 v[44:47], v37 offset:512\n\t"
      "s_add_u32 m0, %[m0n], 3072\n\t"
      "v_xor_b32 v56, 48, %[l16]\n\t"
      "global_load_lds_dwordx4 v56, s[44:45]\n\t"
      "s_add_u32 s44, s44, 1024\n\t"
      "s_addc_u32 s45, s45, 0\n\t"
      "s_waitcnt lgkmcnt(3)\n\t"
      "v_mfma_f32_32x32x16_bf16 v[64:79], a[76:79], v[52:55], v[64:79]\n\t"
      "ds_read_b128 v[48:51], v38 offset:512\n\t"
      "ds_read_b128 v[92:95], %[qr] offset:11264\n\t"
      "s_waitcnt lgkmcnt(4)\n\t"
      "v_mfma_f32_32x32x16_bf16 v[64:79], a[80:83], v[40:43], v[64:79]\n\t"
      "ds_read_b128 v[52:55], v39 offset:512\n\t"
      "s_add_u32 m0, %[m0n], 4096\n\t"
      "v_xor_b32 v56, 64, %[l16]\n\t"
      "global_load_lds_dwordx4 v56, s[44:45]\n\t"
      "s_add_u32 s44, s44, 1024\n\t"
      "s_addc_u32 s45, s45, 0\n\t"
      "s_waitcnt lgkmcnt(3)\n\t"
      "v_mfma_f32_32x32x16_bf16 v[64:79], a[84:87], v[44:47], v[64:79]\n\t"
      "ds_read_b128 v[40:43], v32 offset:768\n\t"
      "s_waitcnt lgkmcnt(3)\n\t"
      "v_mfma_f32_32x32x16_bf16 v[64:79], a[88:91], v[48:51], v[64:79]\n\t"
      "ds_read_b128 v[44:47], v33 offset:768\n\t"
      "s_add_u32 m0, %[m0n], 5120\n\t"
      "v_xor_b32 v56, 80, %[l16]\n\t"
      "global_load_lds_dwordx4 v56, s[44:45]\n\t"
      "s_add_u32 s44, s44, 1024\n\t"
      "s_addc_u32 s45, s45, 0\n\t"
      "s_waitcnt lgkmcnt(2)\n\t"
      "v_mfma_f32_32x32x16_bf16 v[64:79], a[92:95], v[52:55], v[64:79]\n\t"
      "ds_read_b128 v[48:51], v34 offset:768\n\t"
      "s_waitcnt lgkmcnt(2)\n\t"
      "v_mfma_f32_32x32x16_bf16 v[64:79], a[96:99], v[40:43], v[64:79]\n\t"
      "ds_read_b128 v[52:55], v35 offset:768\n\t"
      "s_add_u32 m0, %[m0n], 6144\n\t"
      "v_xor_b32 v56, 96, %[l16]\n\t"
      "global_load_lds_dwordx4 v56, s[44:45]\n\t"
      "s_add_u32 s44, s44, 1024\n\t"
      "s_addc_u32 s45, s45, 0\n\t"
      "s_waitcnt lgkmcnt(2)\n\t"
      "v_mfma_f32_32x32x16_bf16 v[64:79], a[100:103], v[44:47], v[64:79]\n\t"
      "ds_read_b128 v[40:43], v36 offset:768\n\t"
      "s_waitcnt lgkmcnt(2)\n\t"
      "v_mfma_f32_32x32x16_bf16 v[64:79], a[104:107], v[48:51], v[64:79]\n\t"
      "ds_read_b128 v[44:47], v37 offset:768\n\t"
      "s_add_u32 m0, %[m0n], 7168\n\t"
      "v_xor_b32 v56, 112, %[l16]\n\t"
      "global_load_lds_dwordx4 v56, s[44:45]\n\t"
      "s_add_u32 s44, s44, 1024\n\t"
      "s_addc_u32 s45, s45, 0\n\t"
      "s_waitcnt lgkmcnt(2)\n\t"
      "v_mfma_f32_32x32x16_bf16 v[64:79], a[108:111], v[52:55], v[64:79]\n\t"
      "ds_read_b128 v[48:51], v38 offset:768\n\t"
      "s_waitcnt lgkmcnt(2)\n\t"
      "v_mfma_f32_32x32x16_bf16 v[64:79], a[112:115], v[40:43], v[64:79]\n\t"
      "ds_read_b128 v[52:55], v39 offset:768\n\t"
      "s_waitcnt lgkmcnt(2)\n\t"
      "v_mfma_f32_32x32x16_bf16 v[64:79], a[116:119], v[44:47], v[64:79]\n\t"
      "s_waitcnt lgkmcnt(1)\n\t"
      "v_mfma_f32_32x32x16_bf16 v[64:79], a[120:123], v[48:51], v[64:79]\n\t"
      "s_waitcnt lgkmcnt(0)\n\t"
      "v_mfma_f32_32x32x16_bf16 v[64:79], a[124:127], v[52:55], v[64:79]\n\t"
      "v_mov_b32 v186, 0\n\t"
      "v_mov_b32 v187, 0\n\t"
      "v_mov_b32 v230, 0\n\t"
      "v_mov_b32 v231, 0\n\t"
      "s_nop 7\n\t"
      "v_cvt_pk_bf16_f32 v220, v64, v65\n\t"
      "v_cvt_pk_bf16_f32 v221, v66, v67\n\t"
      "v_cvt_pk_bf16_f32 v222, v72, v73\n\t"
      "v_cvt_pk_bf16_f32 v223, v74, v75\n\t"
      "ds_write_b128 %[qw], v[220:223] offset:0\n\t"
      "v_cvt_pk_bf16_f32 v224, v68, v69\n\t"
      "v_cvt_pk_bf16_f32 v225, v70, v71\n\t"
      "v_cvt_pk_bf16_f32 v226, v76, v77\n\t"
      "v_cvt_pk_bf16_f32 v227, v78, v79\n\t"
      "ds_write_b128 %[qw], v[224:227] offset:1024\n\t"
      "s_waitcnt vmcnt(8) lgkmcnt(0)\n\t"
      "s_barrier\n\t"
      "buffer_store_dword %[one], %[gof], %[srd], %[sof] offen sc1\n\t"
      "ds_read_b128 v[188:191], %[qr] offset:0\n\t"
      "ds_read_b128 v[112:115], %[kr] offset:12288\n\t"
      "ds_read_b128 v[192:195], %[qr] offset:1024\n\t"
      "ds_read_b128 v[116:119], %[kr] offset:12800\n\t"
      "ds_read_b128 v[196:199], %[qr] offset:2048\n\t"
      "ds_read_b128 v[120:123], %[kr] offset:13312\n\t"
      "ds_read_b128 v[200:203], %[qr] offset:3072\n\t"
      "ds_read_b128 v[124:127], %[kr] offset:13824\n\t"
      "ds_read_b128 v[204:207], %[qr] offset:4096\n\t"
      "ds_read_b128 v[128:131], %[kr] offset:14336\n\t"
      "ds_read_b128 v[208:211], %[qr] offset:5120\n\t"
      "ds_read_b128 v[132:135], %[kr] offset:14848\n\t"
      "ds_read_b128 v[212:215], %[qr] offset:6144\n\t"
      "ds_read_b128 v[136:139], %[kr] offset:15360\n\t"
      "ds_read_b128 v[216:219], %[qr] offset:7168\n\t"
      "ds_read_b128 v[140:143], %[kr] offset:15872\n\t"
      "s_waitcnt lgkmcnt(14)\n\t"
      "v_mfma_f32_32x32x16_bf16 v[80:95], v[112:115], v[188:191], v[80:95]\n\t"
      "s_waitcnt lgkmcnt(12)\n\t"
      "v_mfma_f32_32x32x16_bf16 v[80:95], v[116:119], v[192:195], v[80:95]\n\t"
      "s_waitcnt lgkmcnt(10)\n\t"
      "v_mfma_f32_32x32x16_bf16 v[80:95], v[120:123], v[196:199], v[80:95]\n\t"
      "s_waitcnt lgkmcnt(8)\n\t"
      "v_mfma_f32_32x32x16_bf16 v[80:95], v[124:127], v[200:203], v[80:95]\n\t"
      "L_w_%=:\n\t"
      "v_cmp_gt_i32 vcc, %[seg], v57\n\t"
      "s_cbranch_vccz L_go_%=\n\t"
      "s_add_u32 %[spin], %[spin], 1\n\t"
      "s_cmp_gt_u32 %[spin], 100000\n\t"
      "s_cbranch_scc1 L_go_%=\n\t"
      "s_sleep 2\n\t"
      "buffer_load_dword v57, %[gf], %[srd], %[skf] offen sc1\n\t"
      "s_waitcnt vmcnt(0)\n\t"
      "s_branch L_w_%=\n\t"
      "L_go_%=:\n\t"
      "s_cmp_ge_i32 %[early], %[seg]\n\t"
      "s_cbranch_scc1 L_have_%=\n\t"
      "s_add_u32 m0, %[mq], 20480\n\t"
      "s_mov_b32 exec_hi, 0\n\t"
      "buffer_load_dwordx4 %[gq], %[srd], %[skm] offen sc1 lds\n\t"
      "s_mov_b32 exec_hi, -1\n\t"
      "buffer_load_dword v184, %[gvm], %[srd], %[skm] offen offset:2048 sc1\n\t"
      "buffer_load_dword v185, %[gvm], %[srd], %[skm] offen offset:2056 sc1\n\t"
      "s_waitcnt lgkmcnt(6)\n\t"
      "v_mfma_f32_32x32x16_bf16 v[80:95], v[128:131], v[204:207], v[80:95]\n\t"
      "s_waitcnt lgkmcnt(4)\n\t"
      "v_mfma_f32_32x32x16_bf16 v[80:95], v[132:135], v[208:211], v[80:95]\n\t"
      "s_waitcnt lgkmcnt(2)\n\t"
      "v_mfma_f32_32x32x16_bf16 v[80:95], v[136:139], v[212:215], v[80:95]\n\t"
      "s_waitcnt lgkmcnt(0)\n\t"
      "v_mfma_f32_32x32x16_bf16 v[80:95], v[140:143], v[216:219], v[80:95]\n\t"
      "s_waitcnt vmcnt(0)\n\t"
      "s_branch L_have2_%=\n\t"
      "L_have_%=:\n\t"
      "s_waitcnt lgkmcnt(0)\n\t"
      "v_mfma_f32_32x32x16_bf16 v[80:95], v[128:131], v[204:207], v[80:95]\n\t"
      "v_mfma_f32_32x32x16_bf16 v[80:95], v[132:135], v[208:211], v[80:95]\n\t"
      "v_mfma_f32_32x32x16_bf16 v[80:95], v[136:139], v[212:215], v[80:95]\n\t"
      "v_mfma_f32_32x32x16_bf16 v[80:95], v[140:143], v[216:219], v[80:95]\n\t"
      "L_have2_%=:\n\t"
      "s_barrier\n\t"
      "s_mov_b32 m0, s42\n\t"
      "v_readlane_b32 %[early], v57, 0\n\t"
      "v_readlane_b32 s40, v57, 1\n\t"
      "ds_read_b128 v[152:155], %[kmr] offset:20480\n\t"
      "ds_read_b128 v[156:159], %[kmr] offset:20736\n\t"
      "ds_read_b128 v[160:163], %[kmr] offset:20992\n\t"
      "ds_read_b128 v[164:167], %[kmr] offset:21248\n\t"
      "ds_read_b128 v[168:171], %[kmr] offset:21504\n\t"
      "ds_read_b128 v[172:175], %[kmr] offset:21760\n\t"
      "ds_read_b128 v[176:179], %[kmr] offset:22016\n\t"
      "ds_read_b128 v[180:183], %[kmr] offset:22272\n\t"
      "s_min_i32 %[early], %[early], s40\n\t"
      "s_waitcnt lgkmcnt(7)\n\t"
      "v_mfma_f32_32x32x16_bf16 v[96:111], v[152:155], v[188:191], 0\n\t"
      "s_waitcnt lgkmcnt(6)\n\t"
      "v_mfma_f32_32x32x16_bf16 v[96:111], v[156:159], v[192:195], v[96:111]\n\t"
      "v_max3_f32 v56, v80, v81, v82\n\t"
      "v_max3_f32 v56, v56, v83, v84\n\t"
      "v_max3_f32 v56, v56, v85, v86\n\t"
      "v_max3_f32 v56, v56, v87, v88\n\t"
      "s_waitcnt lgkmcnt(5)\n\t"
      "v_mfma_f32_32x32x16_bf16 v[96:111], v[160:163], v[196:199], v[96:111]\n\t"
      "v_max3_f32 v56, v56, v89, v90\n\t"
      "v_max3_f32 v56, v56, v91, v92\n\t"
      "v_max3_f32 v56, v56, v93, v94\n\t"
      "v_max_f32 v56, v56, v95\n\t"
      "s_waitcnt lgkmcnt(4)\n\t"
      "v_mfma_f32_32x32x16_bf16 v[96:111], v[164:167], v[200:203], v[96:111]\n\t"
      "s_waitcnt lgkmcnt(3)\n\t"
      "v_mfma_f32_32x32x16_bf16 v[96:111], v[168:171], v[204:207], v[96:111]\n\t"
      "s_waitcnt lgkmcnt(2)\n\t"
      "v_mfma_f32_32x32x16_bf16 v[96:111], v[172:175], v[208:211], v[96:111]\n\t"
      "s_waitcnt lgkmcnt(1)\n\t"
      "v_mfma_f32_32x32x16_bf16 v[96:111], v[176:179], v[212:215], v[96:111]\n\t"
      "s_waitcnt lgkmcnt(0)\n\t"
      "v_mfma_f32_32x32x16_bf16 v[96:111], v[180:183], v[216:219], v[96:111]\n\t"
      "s_nop 11\n\t"
      "v_max3_f32 v56, v56, v96, v97\n\t"
      "v_max3_f32 v56, v56, v98, v99\n\t"
      "v_mov_b32 v57, v56\n\t"
      "s_nop 1\n\t"
      "v_permlane32_swap_b32 v56, v57\n\t"
      "v_max_f32 v56, v56, v57\n\t"
      "v_sub_f32 v80, v80, v56\n\t"
      "v_sub_f32 v81, v81, v56\n\t"
      "v_sub_f32 v82, v82, v56\n\t"
      "v_sub_f32 v83, v83, v56\n\t"
      "v_sub_f32 v88, v88, v56\n\t"
      "v_sub_f32 v89, v89, v56\n\t"
      "v_sub_f32 v90, v90, v56\n\t"
      "v_sub_f32 v91, v91, v56\n\t"
      "v_mul_f32 v80, s41, v80\n\t"
      "v_mul_f32 v81, s41, v81\n\t"
      "v_mul_f32 v82, s41, v82\n\t"
      "v_mul_f32 v83, s41, v83\n\t"
      "v_mul_f32 v88, s41, v88\n\t"
      "v_mul_f32 v89, s41, v89\n\t"
      "v_mul_f32 v90, s41, v90\n\t"
      "v_mul_f32 v91, s41, v91\n\t"
      "v_exp_f32 v80, v80\n\t"
      "v_exp_f32 v81, v81\n\t"
      "v_exp_f32 v82, v82\n\t"
      "v_exp_f32 v83, v83\n\t"
      "v_exp_f32 v88, v88\n\t"
      "v_exp_f32 v89, v89\n\t"
      "v_exp_f32 v90, v90\n\t"
      "v_exp_f32 v91, v91\n\t"
      "v_cvt_pk_bf16_f32 v220, v80, v81\n\t"
      "v_cvt_pk_bf16_f32 v221, v82, v83\n\t"
      "v_cvt_pk_bf16_f32 v222, v88, v89\n\t"
      "v_cvt_pk_bf16_f32 v223, v90, v91\n\t"
      "v_sub_f32 v84, v84, v56\n\t"
      "v_sub_f32 v85, v85, v56\n\t"
      "v_sub_f32 v86, v86, v56\n\t"
      "v_sub_f32 v87, v87, v56\n\t"
      "v_sub_f32 v92, v92, v56\n\t"
      "v_sub_f32 v93, v93, v56\n\t"
      "v_sub_f32 v94, v94, v56\n\t"
      "v_sub_f32 v95, v95, v56\n\t"
      "v_mfma_f32_32x32x16_bf16 v[64:79], v[144:147], v[220:223], 0\n\t"
      "v_mul_f32 v84, s41, v84\n\t"
      "v_mul_f32 v85, s41, v85\n\t"
      "v_mul_f32 v86, s41, v86\n\t"
      "v_mul_f32 v87, s41, v87\n\t"
      "v_mul_f32 v92, s41, v92\n\t"
      "v_mul_f32 v93, s41, v93\n\t"
      "v_mul_f32 v94, s41, v94\n\t"
      "v_mul_f32 v95, s41, v95\n\t"
      "v_exp_f32 v84, v84\n\t"
      "v_exp_f32 v85, v85\n\t"
      "v_exp_f32 v86, v86\n\t"
      "v_exp_f32 v87, v87\n\t"
      "v_exp_f32 v92, v92\n\t"
      "v_exp_f32 v93, v93\n\t"
      "v_exp_f32 v94, v94\n\t"
      "v_exp_f32 v95, v95\n\t"
      "v_cvt_pk_bf16_f32 v224, v84, v85\n\t"
      "v_cvt_pk_bf16_f32 v225, v86, v87\n\t"
      "v_cvt_pk_bf16_f32 v226, v92, v93\n\t"
      "v_cvt_pk_bf16_f32 v227, v94, v95\n\t"
      "v_sub_f32 v96, v96, v56\n\t"
      "v_sub_f32 v97, v97, v56\n\t"
      "v_sub_f32 v98, v98, v56\n\t"
      "v_sub_f32 v99, v99, v56\n\t"
      "v_mfma_f32_32x32x16_bf16 v[64:79], v[148:151], v[224:227], v[64:79]\n\t"
      "v_mul_f32 v96, s41, v96\n\t"
      "v_mul_f32 v97, s41, v97\n\t"
      "v_mul_f32 v98, s41, v98\n\t"
      "v_mul_f32 v99, s41, v99\n\t"
      "v_exp_f32 v96, v96\n\t"
      "v_exp_f32 v97, v97\n\t"
      "v_exp_f32 v98, v98\n\t"
      "v_exp_f32 v99, v99\n\t"
      "v_add_f32 v58, v80, v81\n\t"
      "v_cvt_pk_bf16_f32 v228, v96, v97\n\t"
      "v_cvt_pk_bf16_f32 v229, v98, v99\n\t"
      "v_add_f32 v59, v82, v83\n\t"
      "v_add_f32 v58, v58, v84\n\t"
      "v_mfma_f32_32x32x16_bf16 v[64:79], v[184:187], v[228:231], v[64:79]\n\t"
      "v_add_f32 v59, v59, v85\n\t"
      "v_add_f32 v58, v58, v86\n\t"
      "v_add_f32 v59, v59, v87\n\t"
      "v_add_f32 v58, v58, v88\n\t"
      "v_add_f32 v59, v59, v89\n\t"
      "v_add_f32 v58, v58, v90\n\t"
      "v_add_f32 v59, v59, v91\n\t"
      "v_add_f32 v58, v58, v92\n\t"
      "v_add_f32 v59, v59, v93\n\t"
      "v_add_f32 v58, v58, v94\n\t"
      "v_add_f32 v59, v59, v95\n\t"
      "v_add_f32 v58, v58, v96\n\t"
      "v_add_f32 v59, v59, v97\n\t"
      "v_add_f32 v58, v58, v98\n\t"
      "v_add_f32 v59, v59, v99\n\t"
      "v_add_f32 v58, v58, v59\n\t"
      "v_mov_b32 v59, v58\n\t"
      "s_nop 1\n\t"
      "v_permlane32_swap_b32 v58, v59\n\t"
      "v_add_f32 v58, v58, v59\n\t"
      "v_rcp_f32 v58, v58\n\t"
      "s_nop 0\n\t"
      "v_mul_f32 v64, v64, v58\n\t"
      "v_mul_f32 v65, v65, v58\n\t"
      "v_mul_f32 v66, v66, v58\n\t"
      "v_mul_f32 v67, v67, v58\n\t"
      "v_mul_f32 v68, v68, v58\n\t"
      "v_mul_f32 v69, v69, v58\n\t"
      "v_mul_f32 v70, v70, v58\n\t"
      "v_mul_f32 v71, v71, v58\n\t"
      "v_cvt_pk_bf16_f32 v220, v64, v65\n\t"
      "v_cvt_pk_bf16_f32 v221, v66, v67\n\t"
      "v_cvt_pk_bf16_f32 v222, v68, v69\n\t"
      "v_cvt_pk_bf16_f32 v223, v70, v71\n\t"
      "buffer_store_dwordx4 v[220:223], %[gox], %[srd], %[sox] offen offset:0 sc1\n\t"
      "v_mul_f32 v72, v72, v58\n\t"
      "v_mul_f32 v73, v73, v58\n\t"
      "v_mul_f32 v74, v74, v58\n\t"
      "v_mul_f32 v75, v75, v58\n\t"
      "v_mul_f32 v76, v76, v58\n\t"
      "v_mul_f32 v77, v77, v58\n\t"
      "v_mul_f32 v78, v78, v58\n\t"
      "v_mul_f32 v79, v79, v58\n\t"
      "v_cvt_pk_bf16_f32 v224, v72, v73\n\t"
      "v_cvt_pk_bf16_f32 v225, v74, v75\n\t"
      "v_cvt_pk_bf16_f32 v226, v76, v77\n\t"
      "v_cvt_pk_bf16_f32 v227, v78, v79\n\t"
      "buffer_store_dwordx4 v[224:227], %[gox], %[srd], %[sox] offen offset:1024 sc1\n\t"
      "s_nop 1\n\t"
      : [spin] "+s"(spin), [early] "+s"(early)
      : [xa] "v"(xa), [kr] "v"(kr), [gv] "v"(gv), [l16] "v"(l16), [qr] "v"(qr), [qw] "v"(qw), [kmr] "v"(kmr), [gq] "v"(gq), [gvm] "v"(gvm), [gox] "v"(gox), [gf] "v"(gf), [gof] "v"(gof), [one] "v"(one),
        [srd] "s"(srd), [skv] "s"(skv), [xn] "s"(xn), [m0n] "s"(m0n), [skm] "s"(skm), [sox] "s"(sox), [skf] "s"(skf), [sof] "s"(sof), [seg] "s"(s), [mk] "s"(mk), [mq] "s"(mq),
        "{a[0:31]}"(W[0]), "{a[32:63]}"(W[1]), "{a[64:95]}"(W[2]), "{a[96:127]}"(W[3])
      : XW_CLOB_S, QA_CLOB);
    if (s >= 60) {
      VM_LDS_BARRIER(0);
      if (w == 0 && fresh_lane() == 0) gst4(ws, sof, (unsigned)s);
    }
    prev = s; s = nxt; buf ^= 1;
  }
}

#define PROBE_N 3
#define OW_DEPTH 2
#define OW_SLEEP 0
#define OW_SPLIT 44
#define OW_TSLEEP 2
#define OW_HALF 1
template <int DEPTH, int SLEEP>
DI f32x16 out_dot(rsrc_t ws, unsigned ox, const bf16x8 (&W)[64]) {
  f32x16 acc;
#pragma unroll
  for (int i = 0; i < 16; ++i) acc[i] = 0.f;
  u32x4 b[DEPTH];
#pragma unroll
  for (int i = 0; i < DEPTH; ++i) b[i] = gld16(ws, ox + i * 1024);
  SBAR();
#pragma unroll
  for (int i = 0; i < 64; ++i) {
    acc = mfma32(W[i], as_bf8(b[i % DEPTH]), acc); SBAR();
    if (i + DEPTH < 64) { b[i % DEPTH] = gld16(ws, ox + (DEPTH + i) * 1024); SBAR(); }
    if (SLEEP) __builtin_amdgcn_s_sleep(SLEEP);
  }
  return acc;
}

DI void out_dot_half(rsrc_t ws, unsigned ox, const bf16x8 (&W)[64], int grp, lptr dst) {
  f32x16 a[2];
#pragma unroll
  for (int i = 0; i < 2; ++i)
#pragma unroll
    for (int r = 0; r < 16; ++r) a[i][r] = 0.f;
  u32x4 b[32];
#pragma unroll
  for (int j = 0; j < 32; ++j) b[j] = gld16(ws, ox + j * 2048);
  SBAR();
#pragma unroll
  for (int j = 0; j < 32; ++j) { a[0] = mfma32(W[2 * j], as_bf8(b[j]), a[0]); a[1] = mfma32(W[2 * j + 1], as_bf8(b[j]), a[1]); SBAR(); }
#pragma unroll
  for (int i = 0; i < 2; ++i) {
    if (grp == i) {
#pragma unroll
      for (int b4 = 0; b4 < 4; ++b4) *(LAS f32x4*)(dst + b4 * 32) = (f32x4){a[i][4 * b4], a[i][4 * b4 + 1], a[i][4 * b4 + 2], a[i][4 * b4 + 3]};
    }
    MEMCLOB();
  }
}

DI void init_exchange(const Params& p, rsrc_t ws, int slot, Tag tag) {
  const int lane = threadIdx.x & 63;
  const u32x4 pz = {POISON, POISON, POISON, POISON};
#pragma unroll
  for (int i = 0; i < 32; ++i) gst16(ws, (unsigned)(slot * 32768 + i * 1024 + lane * 16), pz);
  if (slot == 0 && lane < 8) gst4(ws, F_PRB + (unsigned)((lane >> 2) * 8192 + (lane & 3) * 128), POISON);
  if (slot == 1) for (unsigned o = F_OXF + lane * 16; o < F_INI_END; o += 1024) gst16(ws, o, pz);
  VMWAIT(0);
  if (lane == 0) gst8(ws, F_RDY + slot * 8, (u32x2){tag.lo, tag.hi});
  if (slot == 0) {
    unsigned best0 = ~0u, best1 = ~0u;
#pragma unroll 1
    for (int i = 0; i < PROBE_N; ++i) {
      unsigned long long c0, c1, c2; unsigned x, y;
      const unsigned o0 = F_PRB + (unsigned)i * 128, o1 = o0 + 8192;
      asm volatile("s_memtime %0\n\ts_waitcnt lgkmcnt(0)\n\tglobal_load_dword %3, %5, %7 sc1\n\ts_waitcnt vmcnt(0)\n\ts_memtime %1\n\ts_waitcnt lgkmcnt(0)\n\t"
                   "global_load_dword %4, %6, %7 sc1\n\ts_waitcnt vmcnt(0)\n\ts_memtime %2\n\ts_waitcnt lgkmcnt(0)"
                   : "=&s"(c0), "=&s"(c1), "=&s"(c2), "=&v"(x), "=&v"(y) : "v"(o0), "v"(o1), "s"(p.ws) : "memory");
      const unsigned d0 = (unsigned)(c1 - c0), d1 = (unsigned)(c2 - c1);
      best0 = d0 < best0 ? d0 : best0; best1 = d1 < best1 ? d1 : best1;
    }
    wait_tag(ws, F_RDY + lane * 8, tag);
    if (lane == 0) gst8(ws, F_SEL, (u32x2){tag.lo + (best1 < best0 ? 1u : 0u), tag.hi});
#pragma unroll 1
    for (int c = 0; c < 4; ++c) wait_tag(ws, F_CLM + (unsigned)(64 * c + lane) * 64, tag);
    if (lane == 0) gst8(ws, F_EPO, (u32x2){tag.lo, tag.hi});
  }
}

DI void out_worker(const Params& p, rsrc_t ws, lptr lds, int cg, int sg, bool gentle, Tag tag) {
  const int w = rfl(threadIdx.x >> 6), lane = fresh_lane(), tid = w * 64 + lane;
  const int r32 = lane & 31, hh = lane >> 5, q4 = (lane & 15) >> 2, pp = lane & 3;
  const unsigned lds0 = (unsigned)(size_t)lds;
  if (gentle) {
    wait_tag(ws, F_SEL, tag);
    for (int i = 0; i < 3; ++i) __builtin_amdgcn_s_sleep(32);
    VM_LDS_BARRIER(0);
  }
  if (gentle && OW_SPLIT == 0) return;
  bf16x8 W[64];
  {
    auto issue = [&](int j) {
#pragma unroll
      for (int i = 0; i < 8; ++i) {
        dma16(p.wo, (unsigned)(((8 * w + j) * 128 + 16 * i + (lane >> 2)) * 1024 + cg * 64 + (lane & 3) * 16), (unsigned)rfl(lds0 + w * 40960 + (j % 5) * 8192 + i * 1024));
        if (gentle) VMWAIT(1);
      }
    };
    issue(0); issue(1); issue(2); issue(3);
    const int cb = w * 40960 + (4 * hh + q4) * 64 + ((lane >> 4) & 1) * 32 + pp * 8;
#pragma unroll
    for (int j = 0; j < 8; ++j) {
      if (j <= 4) asm volatile("s_waitcnt vmcnt(24) lgkmcnt(0)" ::: "memory"); else if (j == 5) asm volatile("s_waitcnt vmcnt(16) lgkmcnt(0)" ::: "memory");
      else if (j == 6) asm volatile("s_waitcnt vmcnt(8) lgkmcnt(0)" ::: "memory"); else asm volatile("s_waitcnt vmcnt(0) lgkmcnt(0)" ::: "memory");
      if (j + 4 < 8) issue(j + 4);
#pragma unroll
      for (int f = 0; f < 8; ++f) { lptr b = lds + (j % 5) * 8192 + f * 1024 + cb; W[8 * j + f] = tr2(b, b + 512); }
    }
    LDS_BARRIER();
  }
  int par = 0;
  for (int s = (gentle ? 0 : OW_SPLIT) + sg; s < (gentle ? OW_SPLIT : 64);) {
    const bool quarter = s >= 62 && OW_HALF;
    wait_ge(ws, F_OXF + (unsigned)((8 * w + (lane & 7)) * 128 + (s > QA_SPLIT && (s & 1) ? 64 : 0)), s, quarter ? OW_TSLEEP : 8);
    const unsigned ox = OX_OFF + (unsigned)((s * 32 + 8 * w) * 8192);
    lptr red = lds + par * 20480, dst = red + w * 4608 + r32 * 144 + hh * 16;
    par ^= 1;
    const int tok = tid >> 3, c4 = tid & 7;
    if (quarter) {
      out_dot_half(ws, ox + (unsigned)((r32 >> 4) * 1024 + (hh * 32 + 16 * (sg & 1) + (r32 & 15)) * 16), W, r32 >> 4, dst);
      LDS_BARRIER();
      if (w < 2) {
        f32x4 t = {0.f, 0.f, 0.f, 0.f};
#pragma unroll
        for (int v = 0; v < 8; ++v) t += *(LAS f32x4*)(red + (v >> 1) * 4608 + (16 * (v & 1) + tok) * 144 + c4 * 16);
        *(u32x2*)(p.out + (size_t)(32 * s + 16 * (sg & 1) + tok) * 1024 + 32 * cg + 4 * c4) = (u32x2){pack2(t[0], t[1]), pack2(t[2], t[3])};
      }
    } else {
      f32x16 acc;
      if (gentle) acc = out_dot<OW_DEPTH, OW_SLEEP>(ws, ox + lane * 16, W); else acc = out_dot<32, 0>(ws, ox + lane * 16, W);
#pragma unroll
      for (int b = 0; b < 4; ++b) *(LAS f32x4*)(dst + b * 32) = (f32x4){acc[4 * b], acc[4 * b + 1], acc[4 * b + 2], acc[4 * b + 3]};
      LDS_BARRIER();
      f32x4 t = *(LAS f32x4*)(red + tok * 144 + c4 * 16);
#pragma unroll
      for (int v = 1; v < 4; ++v) t += *(LAS f32x4*)(red + v * 4608 + tok * 144 + c4 * 16);
      *(u32x2*)(p.out + (size_t)(32 * s + tok) * 1024 + 32 * cg + 4 * c4) = (u32x2){pack2(t[0], t[1]), pack2(t[2], t[3])};
    }
    s = (gentle || !OW_HALF || s < 56 || s == 56 || s == 57) ? s + 4 : (s < 60 ? 62 : (s < 62 ? 63 : 64));
  }
}

constexpr int B_K = 0, B_V = 65536, B_C = 131072;
constexpr float C_B = 0.25f * 1.4426950408889634f;

DI void b_out(const Params& p, rsrc_t ws, lptr lds, int hb, int half, int w) {
  const int lane = fresh_lane(), tid = w * 64 + lane;
  const int r32 = lane & 31, hh = lane >> 5, q4 = (lane & 15) >> 2, pp = lane & 3, g1 = (lane >> 4) & 1;
  const unsigned lds0 = (unsigned)(size_t)lds;
  f32x16 z16;
#pragma unroll
  for (int i = 0; i < 16; ++i) z16[i] = 0.f;
  VM_LDS_BARRIER(0);
  if (tid == 0) gst4(ws, F_OBF + (unsigned)((half * 32 + hb) * 4), 1u);
  const int tg = half * 8 + (hb >> 2), cq = hb & 3;
#pragma unroll
  for (int c = 0; c < 4; ++c) stage128(p.bwo, (unsigned)((128 * c * 512 + 128 * cq) * 2), 1024u, lds0 + c * 32768, w, lane);
  wait_ge(ws, F_OBF + (unsigned)((half * 32 + r32) * 4), 0, 8);
  const int T = 4 * tg + w;
  u32x4 of[32];
#pragma unroll
  for (int k = 0; k < 32; ++k) of[k] = gld16(ws, OB_OFF + (unsigned)((T * 32 + k) * 1024 + lane * 16));
  VM_LDS_BARRIER(0);
#pragma unroll
  for (int ct = 0; ct < 4; ++ct) {
    f32x16 acc = z16;
    lptr b = lds + (4 * hh + q4) * 256 + ((4 * ct + 2 * g1 + (pp >> 1)) ^ (q4 << 2)) * 16 + 8 * (pp & 1);
#pragma unroll
    for (int k = 0; k < 32; ++k) acc = mfma32(tr2(b + k * 4096, b + k * 4096 + 2048), as_bf8(of[k]), acc);
    __bf16* op = p.out + (size_t)(32 * T + r32) * 1024 + 512 + 128 * cq + 32 * ct + 4 * hh;
#pragma unroll
    for (int bb = 0; bb < 4; ++bb) *(u32x2*)(op + 8 * bb) = (u32x2){pack2(acc[4 * bb], acc[4 * bb + 1]), pack2(acc[4 * bb + 2], acc[4 * bb + 3])};
  }
}

DI void b_worker(const Params& p, rsrc_t ws, lptr lds, int hb, int half, Tag tag) {
  const int tid = threadIdx.x, lane = tid & 63, w = rfl(tid >> 6);
  const int r32 = lane & 31, hh = lane >> 5, q4 = (lane & 15) >> 2, pp = lane & 3, g1 = (lane >> 4) & 1;
  const unsigned lds0 = (unsigned)(size_t)lds;
  f32x16 z16;
#pragma unroll
  for (int i = 0; i < 16; ++i) z16[i] = 0.f;
  const int rot = hb & 7;
  bf16x8 Qf[8];
#pragma unroll
  for (int j = 0; j < 8; ++j) Qf[j] = as_bf8((u32x4){0u, 0u, 0u, 0u});
  {
#define BW_DELAY 6
    for (int i = 0; i < BW_DELAY; ++i) __builtin_amdgcn_s_sleep(32);
    wait_tag(ws, F_SEL, tag);
    u32x4 wst[12];
#pragma unroll
    for (int i = 0; i < 12; ++i) {
      const int id = tid + 256 * i, row = id / 6, pc = id % 6, seg = pc >> 1;
      wst[i] = *(const u32x4*)(p.bwqkv + (size_t)row * 1536 + (seg == 0 ? 512 : (seg == 1 ? 0 : 1024)) + 16 * hb + 8 * (pc & 1));
    }
    SBAR();
    {
      const int tile = 2 * hb + half;
      const unsigned l16 = (unsigned)((lane ^ (8 * (w & 1))) * 16);
#pragma unroll
      for (int i = 0; i < 8; ++i) dma16(p.bx + (size_t)(32 * tile + 8 * w + i) * 512, l16 ^ (unsigned)(i * 16), (unsigned)rfl(lds0 + 65536 + (8 * w + i) * 1024));
      VM_LDS_BARRIER(0);
#pragma unroll
      for (int i = 0; i < 8; ++i) {
        const int kk = 8 * w + i;
        gst16(ws, XB_OFF + (unsigned)((tile * 32 + kk) * 1024 + lane * 16), lds_ld16(lds + 65536 + r32 * 1024 + w * 256 + (((2 * i + hh) ^ (r32 & 15)) * 16)));
      }
      VM_LDS_BARRIER(0);
      if (tid == 0) gst4(ws, F_XBF + (unsigned)tile * 4, 1u);
    }
#pragma unroll
    for (int i = 0; i < 12; ++i) { const int id = tid + 256 * i; lds_st16(lds + (id / 6) * 96 + (id % 6) * 16, wst[i]); }
    LDS_BARRIER();
    bf16x8 Wkq[32], Wv[32];
#pragma unroll
    for (int kk = 0; kk < 32; ++kk) {
      lptr b = lds + (16 * kk + 8 * hh + q4) * 96 + pp * 8;
      Wkq[kk] = tr2(b + g1 * 32, b + g1 * 32 + 4 * 96);
      Wv[kk] = tr2(b + 64, b + 64 + 4 * 96);
    }
    LDS_BARRIER();
    if (tid < 128) lds_st16(lds + B_C + tid * 16, tid < 64 ? (u32x4){0x3F803F80u, 0x3F803F80u, 0x3F803F80u, 0x3F803F80u} : (u32x4){0u, 0u, 0u, 0u});
    {
      wait_ge(ws, F_XBF + (unsigned)(32 * half + r32) * 4, 0, 8);
      u32x4 xf[16];
      unsigned xp = XB_OFF + (unsigned)((32 * half + 4 * rot + w) * 32768 + lane * 16);
#pragma unroll
      for (int k = 0; k < 16; ++k) xf[k] = gld16(ws, xp + k * 1024);
#pragma unroll 1
      for (int it = 0; it < 8; ++it) {
        const int T = 32 * half + 4 * ((it + rot) & 7) + w;
        const unsigned xn = XB_OFF + (unsigned)((32 * half + 4 * ((it + 1 + rot) & 7) + w) * 32768 + lane * 16);
        f32x16 akq = z16, av = z16;
        SBAR();
#pragma unroll
        for (int k = 0; k < 32; ++k) {
          akq = mfma32(Wkq[k], as_bf8(xf[k & 15]), akq); av = mfma32(as_bf8(xf[k & 15]), Wv[k], av);
          SBAR();
          xf[k & 15] = k < 16 ? gld16(ws, xp + (k + 16) * 1024) : gld16(ws, xn + (k - 16) * 1024);
          SBAR();
        }
        xp = xn;
        const u32x4 kf = {pack2(akq[0], akq[1]), pack2(akq[2], akq[3]), pack2(akq[4], akq[5]), pack2(akq[6], akq[7])};
        lds_st16(lds + B_K + T * 1024 + lane * 16, kf);
        gst16(ws, BK_OFF + (unsigned)((hb * 128 + T) * 1024 + lane * 16), kf);
#pragma unroll
        for (int j = 0; j < 7; ++j) Qf[j] = Qf[j + 1];
        Qf[7] = as_bf8((u32x4){pack2(akq[8] * C_B, akq[9] * C_B), pack2(akq[10] * C_B, akq[11] * C_B), pack2(akq[12] * C_B, akq[13] * C_B), pack2(akq[14] * C_B, akq[15] * C_B)});
        u32x4 vf = {pack2(av[0], av[1]), pack2(av[2], av[3]), pack2(av[4], av[5]), pack2(av[6], av[7])};
        u32x4 vg = {pack2(av[8], av[9]), pack2(av[10], av[11]), pack2(av[12], av[13]), pack2(av[14], av[15])};
        asm volatile("" : "+v"(vf), "+v"(vg));
#pragma unroll
        for (int j = 0; j < 4; ++j) vf[j] = g1 ? vg[j] : vf[j];
        const unsigned vo = (unsigned)(T * 1024 + g1 * 512 + hh * 256 + (r32 & 15) * 16);
        lds_st16(lds + B_V + vo, vf);
        gst16(ws, BK_OFF + (unsigned)((hb * 128 + 64) * 1024) + vo, vf);
      }
      VM_LDS_BARRIER(0);
      if (tid == 0) gst4(ws, F_BKF + (unsigned)(2 * hb + half) * 4, 1u);
      wait_ge(ws, F_BKF + (unsigned)(2 * hb + (half ^ 1)) * 4, 0, 8);
#pragma unroll
      for (int i = 0; i < 16; ++i) {
        const unsigned o = (unsigned)(((i >> 3) * 64 + 32 * (half ^ 1) + 8 * w + (i & 7)) * 1024);
        dma16_sc1(p.ws, BK_OFF + (unsigned)hb * 131072 + o + lane * 16, (unsigned)rfl(lds0 + o));
      }
    }
    VM_LDS_BARRIER(0);
  }
  {
    unsigned kp = lds0 + B_K + lane * 16;
    unsigned vp = lds0 + (r32 < 16 ? B_V + hh * 256 + r32 * 16 : (r32 == 16 ? B_C : B_C + 1024));
    const unsigned vs = r32 < 16 ? 1024u : 0u, ob = (unsigned)lane * 16;
    const unsigned so = OB_OFF + (unsigned)(((32 * half + w) * 32 + hb) * 1024);
    const size_t wsa = (size_t)p.ws;
    const u32x4 srd = {(unsigned)wsa, (unsigned)(wsa >> 32) & 0xffffu, WS_BYTES, 0x00020000u};
    asm volatile(
      "s_waitcnt vmcnt(0) lgkmcnt(0)\n\t"
      "v_mov_b32 v107, %[kp]\n\t"
      "v_mov_b32 v108, %[vp]\n\t"
      "s_mov_b32 s43, 0\n\t"
      "L_start_%=:\n\t"
      "ds_read_b128 v[112:115], %[kp]\n\t"
      "ds_read_b128 v[120:123], %[vp]\n\t"
      "ds_read_b128 v[124:127], %[vp] offset:512\n\t"
      "v_accvgpr_write_b32 a0, 0\n\t"
      "v_accvgpr_write_b32 a1, 0\n\t"
      "v_accvgpr_write_b32 a2, 0\n\t"
      "v_accvgpr_write_b32 a3, 0\n\t"
      "v_accvgpr_write_b32 a4, 0\n\t"
      "v_accvgpr_write_b32 a5, 0\n\t"
      "v_accvgpr_write_b32 a6, 0\n\t"
      "v_accvgpr_write_b32 a7, 0\n\t"
      "v_accvgpr_write_b32 a8, 0\n\t"
      "v_accvgpr_write_b32 a9, 0\n\t"
      "v_accvgpr_write_b32 a10, 0\n\t"
      "v_accvgpr_write_b32 a11, 0\n\t"
      "v_accvgpr_write_b32 a12, 0\n\t"
      "v_accvgpr_write_b32 a13, 0\n\t"
      "v_accvgpr_write_b32 a14, 0\n\t"
      "v_accvgpr_write_b32 a15, 0\n\t"
      "v_accvgpr_write_b32 a16, 0\n\t"
      "v_accvgpr_write_b32 a17, 0\n\t"
      "v_accvgpr_write_b32 a18, 0\n\t"
      "v_accvgpr_write_b32 a19, 0\n\t"
      "v_accvgpr_write_b32 a20, 0\n\t"
      "v_accvgpr_write_b32 a21, 0\n\t"
      "v_accvgpr_write_b32 a22, 0\n\t"
      "v_accvgpr_write_b32 a23, 0\n\t"
      "v_accvgpr_write_b32 a24, 0\n\t"
      "v_accvgpr_write_b32 a25, 0\n\t"
      "v_accvgpr_write_b32 a26, 0\n\t"
      "v_accvgpr_write_b32 a27, 0\n\t"
      "v_accvgpr_write_b32 a28, 0\n\t"
      "v_accvgpr_write_b32 a29, 0\n\t"
      "v_accvgpr_write_b32 a30, 0\n\t"
      "v_accvgpr_write_b32 a31, 0\n\t"
      "v_accvgpr_write_b32 a32, 0\n\t"
      "v_accvgpr_write_b32 a33, 0\n\t"
      "v_accvgpr_write_b32 a34, 0\n\t"
      "v_accvgpr_write_b32 a35, 0\n\t"
      "v_accvgpr_write_b32 a36, 0\n\t"
      "v_accvgpr_write_b32 a37, 0\n\t"
      "v_accvgpr_write_b32 a38, 0\n\t"
      "v_accvgpr_write_b32 a39, 0\n\t"
      "v_accvgpr_write_b32 a40, 0\n\t"
      "v_accvgpr_write_b32 a41, 0\n\t"
      "v_accvgpr_write_b32 a42, 0\n\t"
      "v_accvgpr_write_b32 a43, 0\n\t"
      "v_accvgpr_write_b32 a44, 0\n\t"
      "v_accvgpr_write_b32 a45, 0\n\t"
      "v_accvgpr_write_b32 a46, 0\n\t"
      "v_accvgpr_write_b32 a47, 0\n\t"
      "v_accvgpr_write_b32 a48, 0\n\t"
      "v_accvgpr_write_b32 a49, 0\n\t"
      "v_accvgpr_write_b32 a50, 0\n\t"
      "v_accvgpr_write_b32 a51, 0\n\t"
      "v_accvgpr_write_b32 a52, 0\n\t"
      "v_accvgpr_write_b32 a53, 0\n\t"
      "v_accvgpr_write_b32 a54, 0\n\t"
      "v_accvgpr_write_b32 a55, 0\n\t"
      "v_accvgpr_write_b32 a56, 0\n\t"
      "v_accvgpr_write_b32 a57, 0\n\t"
      "v_accvgpr_write_b32 a58, 0\n\t"
      "v_accvgpr_write_b32 a59, 0\n\t"
      "v_accvgpr_write_b32 a60, 0\n\t"
      "v_accvgpr_write_b32 a61, 0\n\t"
      "v_accvgpr_write_b32 a62, 0\n\t"
      "v_accvgpr_write_b32 a63, 0\n\t"
      "v_accvgpr_write_b32 a64, 0\n\t"
      "v_accvgpr_write_b32 a65, 0\n\t"
      "v_accvgpr_write_b32 a66, 0\n\t"
      "v_accvgpr_write_b32 a67, 0\n\t"
      "v_accvgpr_write_b32 a68, 0\n\t"
      "v_accvgpr_write_b32 a69, 0\n\t"
      "v_accvgpr_write_b32 a70, 0\n\t"
      "v_accvgpr_write_b32 a71, 0\n\t"
      "v_accvgpr_write_b32 a72, 0\n\t"
      "v_accvgpr_write_b32 a73, 0\n\t"
      "v_accvgpr_write_b32 a74, 0\n\t"
      "v_accvgpr_write_b32 a75, 0\n\t"
      "v_accvgpr_write_b32 a76, 0\n\t"
      "v_accvgpr_write_b32 a77, 0\n\t"
      "v_accvgpr_write_b32 a78, 0\n\t"
      "v_accvgpr_write_b32 a79, 0\n\t"
      "v_accvgpr_write_b32 a80, 0\n\t"
      "v_accvgpr_write_b32 a81, 0\n\t"
      "v_accvgpr_write_b32 a82, 0\n\t"
      "v_accvgpr_write_b32 a83, 0\n\t"
      "v_accvgpr_write_b32 a84, 0\n\t"
      "v_accvgpr_write_b32 a85, 0\n\t"
      "v_accvgpr_write_b32 a86, 0\n\t"
      "v_accvgpr_write_b32 a87, 0\n\t"
      "v_accvgpr_write_b32 a88, 0\n\t"
      "v_accvgpr_write_b32 a89, 0\n\t"
      "v_accvgpr_write_b32 a90, 0\n\t"
      "v_accvgpr_write_b32 a91, 0\n\t"
      "v_accvgpr_write_b32 a92, 0\n\t"
      "v_accvgpr_write_b32 a93, 0\n\t"
      "v_accvgpr_write_b32 a94, 0\n\t"
      "v_accvgpr_write_b32 a95, 0\n\t"
      "v_accvgpr_write_b32 a96, 0\n\t"
      "v_accvgpr_write_b32 a97, 0\n\t"
      "v_accvgpr_write_b32 a98, 0\n\t"
      "v_accvgpr_write_b32 a99, 0\n\t"
      "v_accvgpr_write_b32 a100, 0\n\t"
      "v_accvgpr_write_b32 a101, 0\n\t"
      "v_accvgpr_write_b32 a102, 0\n\t"
      "v_accvgpr_write_b32 a103, 0\n\t"
      "v_accvgpr_write_b32 a104, 0\n\t"
      "v_accvgpr_write_b32 a105, 0\n\t"
      "v_accvgpr_write_b32 a106, 0\n\t"
      "v_accvgpr_write_b32 a107, 0\n\t"
      "v_accvgpr_write_b32 a108, 0\n\t"
      "v_accvgpr_write_b32 a109, 0\n\t"
      "v_accvgpr_write_b32 a110, 0\n\t"
      "v_accvgpr_write_b32 a111, 0\n\t"
      "v_accvgpr_write_b32 a112, 0\n\t"
      "v_accvgpr_write_b32 a113, 0\n\t"
      "v_accvgpr_write_b32 a114, 0\n\t"
      "v_accvgpr_write_b32 a115, 0\n\t"
      "v_accvgpr_write_b32 a116, 0\n\t"
      "v_accvgpr_write_b32 a117, 0\n\t"
      "v_accvgpr_write_b32 a118, 0\n\t"
      "v_accvgpr_write_b32 a119, 0\n\t"
      "v_accvgpr_write_b32 a120, 0\n\t"
      "v_accvgpr_write_b32 a121, 0\n\t"
      "v_accvgpr_write_b32 a122, 0\n\t"
      "v_accvgpr_write_b32 a123, 0\n\t"
      "v_accvgpr_write_b32 a124, 0\n\t"
      "v_accvgpr_write_b32 a125, 0\n\t"
      "v_accvgpr_write_b32 a126, 0\n\t"
      "v_accvgpr_write_b32 a127, 0\n\t"
      "s_waitcnt lgkmcnt(2)\n\t"
      "v_mfma_f32_32x32x16_bf16 v[64:79], v[112:115], %[q0], 0\n\t"
      "s_nop 11\n\t"
      "v_max3_f32 v104, v64, v65, v66\n\t"
      "v_max3_f32 v105, v67, v68, v69\n\t"
      "v_max3_f32 v104, v104, v70, v71\n\t"
      "v_max3_f32 v105, v105, v72, v73\n\t"
      "v_max3_f32 v104, v104, v74, v75\n\t"
      "v_max3_f32 v105, v105, v76, v77\n\t"
      "v_max3_f32 v104, v104, v78, v79\n\t"
      "v_max_f32 v104, v104, v105\n\t"
      "v_mov_b32 v105, v104\n\t"
      "s_nop 1\n\t"
      "v_permlane32_swap_b32 v104, v105\n\t"
      "v_max_f32 v104, v104, v105\n\t"
      "v_sub_f32 v128, 0, v104\n\t"
      "v_sub_f32 v129, 0, v104\n\t"
      "v_sub_f32 v130, 0, v104\n\t"
      "v_sub_f32 v131, 0, v104\n\t"
      "v_sub_f32 v132, 0, v104\n\t"
      "v_sub_f32 v133, 0, v104\n\t"
      "v_sub_f32 v134, 0, v104\n\t"
      "v_sub_f32 v135, 0, v104\n\t"
      "v_sub_f32 v136, 0, v104\n\t"
      "v_sub_f32 v137, 0, v104\n\t"
      "v_sub_f32 v138, 0, v104\n\t"
      "v_sub_f32 v139, 0, v104\n\t"
      "v_sub_f32 v140, 0, v104\n\t"
      "v_sub_f32 v141, 0, v104\n\t"
      "v_sub_f32 v142, 0, v104\n\t"
      "v_sub_f32 v143, 0, v104\n\t"
      "v_mfma_f32_32x32x16_bf16 v[64:79], v[112:115], %[q1], 0\n\t"
      "s_nop 11\n\t"
      "v_max3_f32 v104, v64, v65, v66\n\t"
      "v_max3_f32 v105, v67, v68, v69\n\t"
      "v_max3_f32 v104, v104, v70, v71\n\t"
      "v_max3_f32 v105, v105, v72, v73\n\t"
      "v_max3_f32 v104, v104, v74, v75\n\t"
      "v_max3_f32 v105, v105, v76, v77\n\t"
      "v_max3_f32 v104, v104, v78, v79\n\t"
      "v_max_f32 v104, v104, v105\n\t"
      "v_mov_b32 v105, v104\n\t"
      "s_nop 1\n\t"
      "v_permlane32_swap_b32 v104, v105\n\t"
      "v_max_f32 v104, v104, v105\n\t"
      "v_sub_f32 v144, 0, v104\n\t"
      "v_sub_f32 v145, 0, v104\n\t"
      "v_sub_f32 v146, 0, v104\n\t"
      "v_sub_f32 v147, 0, v104\n\t"
      "v_sub_f32 v148, 0, v104\n\t"
      "v_sub_f32 v149, 0, v104\n\t"
      "v_sub_f32 v150, 0, v104\n\t"
      "v_sub_f32 v151, 0, v104\n\t"
      "v_sub_f32 v152, 0, v104\n\t"
      "v_sub_f32 v153, 0, v104\n\t"
      "v_sub_f32 v154, 0, v104\n\t"
      "v_sub_f32 v155, 0, v104\n\t"
      "v_sub_f32 v156, 0, v104\n\t"
      "v_sub_f32 v157, 0, v104\n\t"
      "v_sub_f32 v158, 0, v104\n\t"
      "v_sub_f32 v159, 0, v104\n\t"
      "v_mfma_f32_32x32x16_bf16 v[64:79], v[112:115], %[q2], 0\n\t"
      "s_nop 11\n\t"
      "v_max3_f32 v104, v64, v65, v66\n\t"
      "v_max3_f32 v105, v67, v68, v69\n\t"
      "v_max3_f32 v104, v104, v70, v71\n\t"
      "v_max3_f32 v105, v105, v72, v73\n\t"
      "v_max3_f32 v104, v104, v74, v75\n\t"
      "v_max3_f32 v105, v105, v76, v77\n\t"
      "v_max3_f32 v104, v104, v78, v79\n\t"
      "v_max_f32 v104, v104, v105\n\t"
      "v_mov_b32 v105, v104\n\t"
      "s_nop 1\n\t"
      "v_permlane32_swap_b32 v104, v105\n\t"
      "v_max_f32 v104, v104, v105\n\t"
      "v_sub_f32 v160, 0, v104\n\t"
      "v_sub_f32 v161, 0, v104\n\t"
      "v_sub_f32 v162, 0, v104\n\t"
      "v_sub_f32 v163, 0, v104\n\t"
      "v_sub_f32 v164, 0, v104\n\t"
      "v_sub_f32 v165, 0, v104\n\t"
      "v_sub_f32 v166, 0, v104\n\t"
      "v_sub_f32 v167, 0, v104\n\t"
      "v_sub_f32 v168, 0, v104\n\t"
      "v_sub_f32 v169, 0, v104\n\t"
      "v_sub_f32 v170, 0, v104\n\t"
      "v_sub_f32 v171, 0, v104\n\t"
      "v_sub_f32 v172, 0, v104\n\t"
      "v_sub_f32 v173, 0, v104\n\t"
      "v_sub_f32 v174, 0, v104\n\t"
      "v_sub_f32 v175, 0, v104\n\t"
      "v_mfma_f32_32x32x16_bf16 v[64:79], v[112:115], %[q3], 0\n\t"
      "s_nop 11\n\t"
      "v_max3_f32 v104, v64, v65, v66\n\t"
      "v_max3_f32 v105, v67, v68, v69\n\t"
      "v_max3_f32 v104, v104, v70, v71\n\t"
      "v_max3_f32 v105, v105, v72, v73\n\t"
      "v_max3_f32 v104, v104, v74, v75\n\t"
      "v_max3_f32 v105, v105, v76, v77\n\t"
      "v_max3_f32 v104, v104, v78, v79\n\t"
      "v_max_f32 v104, v104, v105\n\t"
      "v_mov_b32 v105, v104\n\t"
      "s_nop 1\n\t"
      "v_permlane32_swap_b32 v104, v105\n\t"
      "v_max_f32 v104, v104, v105\n\t"
      "v_sub_f32 v176, 0, v104\n\t"
      "v_sub_f32 v177, 0, v104\n\t"
      "v_sub_f32 v178, 0, v104\n\t"
      "v_sub_f32 v179, 0, v104\n\t"
      "v_sub_f32 v180, 0, v104\n\t"
      "v_sub_f32 v181, 0, v104\n\t"
      "v_sub_f32 v182, 0, v104\n\t"
      "v_sub_f32 v183, 0, v104\n\t"
      "v_sub_f32 v184, 0, v104\n\t"
      "v_sub_f32 v185, 0, v104\n\t"
      "v_sub_f32 v186, 0, v104\n\t"
      "v_sub_f32 v187, 0, v104\n\t"
      "v_sub_f32 v188, 0, v104\n\t"
      "v_sub_f32 v189, 0, v104\n\t"
      "v_sub_f32 v190, 0, v104\n\t"
      "v_sub_f32 v191, 0, v104\n\t"
      "v_mfma_f32_32x32x16_bf16 v[64:79], v[112:115], %[q4], 0\n\t"
      "s_nop 11\n\t"
      "v_max3_f32 v104, v64, v65, v66\n\t"
      "v_max3_f32 v105, v67, v68, v69\n\t"
      "v_max3_f32 v104, v104, v70, v71\n\t"
      "v_max3_f32 v105, v105, v72, v73\n\t"
      "v_max3_f32 v104, v104, v74, v75\n\t"
      "v_max3_f32 v105, v105, v76, v77\n\t"
      "v_max3_f32 v104, v104, v78, v79\n\t"
      "v_max_f32 v104, v104, v105\n\t"
      "v_mov_b32 v105, v104\n\t"
      "s_nop 1\n\t"
      "v_permlane32_swap_b32 v104, v105\n\t"
      "v_max_f32 v104, v104, v105\n\t"
      "v_sub_f32 v192, 0, v104\n\t"
      "v_sub_f32 v193, 0, v104\n\t"
      "v_sub_f32 v194, 0, v104\n\t"
      "v_sub_f32 v195, 0, v104\n\t"
      "v_sub_f32 v196, 0, v104\n\t"
      "v_sub_f32 v197, 0, v104\n\t"
      "v_sub_f32 v198, 0, v104\n\t"
      "v_sub_f32 v199, 0, v104\n\t"
      "v_sub_f32 v200, 0, v104\n\t"
      "v_sub_f32 v201, 0, v104\n\t"
      "v_sub_f32 v202, 0, v104\n\t"
      "v_sub_f32 v203, 0, v104\n\t"
      "v_sub_f32 v204, 0, v104\n\t"
      "v_sub_f32 v205, 0, v104\n\t"
      "v_sub_f32 v206, 0, v104\n\t"
      "v_sub_f32 v207, 0, v104\n\t"
      "v_mfma_f32_32x32x16_bf16 v[64:79], v[112:115], %[q5], 0\n\t"
      "s_nop 11\n\t"
      "v_max3_f32 v104, v64, v65, v66\n\t"
      "v_max3_f32 v105, v67, v68, v69\n\t"
      "v_max3_f32 v104, v104, v70, v71\n\t"
      "v_max3_f32 v105, v105, v72, v73\n\t"
      "v_max3_f32 v104, v104, v74, v75\n\t"
      "v_max3_f32 v105, v105, v76, v77\n\t"
      "v_max3_f32 v104, v104, v78, v79\n\t"
      "v_max_f32 v104, v104, v105\n\t"
      "v_mov_b32 v105, v104\n\t"
      "s_nop 1\n\t"
      "v_permlane32_swap_b32 v104, v105\n\t"
      "v_max_f32 v104, v104, v105\n\t"
      "v_sub_f32 v208, 0, v104\n\t"
      "v_sub_f32 v209, 0, v104\n\t"
      "v_sub_f32 v210, 0, v104\n\t"
      "v_sub_f32 v211, 0, v104\n\t"
      "v_sub_f32 v212, 0, v104\n\t"
      "v_sub_f32 v213, 0, v104\n\t"
      "v_sub_f32 v214, 0, v104\n\t"
      "v_sub_f32 v215, 0, v104\n\t"
      "v_sub_f32 v216, 0, v104\n\t"
      "v_sub_f32 v217, 0, v104\n\t"
      "v_sub_f32 v218, 0, v104\n\t"
      "v_sub_f32 v219, 0, v104\n\t"
      "v_sub_f32 v220, 0, v104\n\t"
      "v_sub_f32 v221, 0, v104\n\t"
      "v_sub_f32 v222, 0, v104\n\t"
      "v_sub_f32 v223, 0, v104\n\t"
      "v_mfma_f32_32x32x16_bf16 v[64:79], v[112:115], %[q6], 0\n\t"
      "s_nop 11\n\t"
      "v_max3_f32 v104, v64, v65, v66\n\t"
      "v_max3_f32 v105, v67, v68, v69\n\t"
      "v_max3_f32 v104, v104, v70, v71\n\t"
      "v_max3_f32 v105, v105, v72, v73\n\t"
      "v_max3_f32 v104, v104, v74, v75\n\t"
      "v_max3_f32 v105, v105, v76, v77\n\t"
      "v_max3_f32 v104, v104, v78, v79\n\t"
      "v_max_f32 v104, v104, v105\n\t"
      "v_mov_b32 v105, v104\n\t"
      "s_nop 1\n\t"
      "v_permlane32_swap_b32 v104, v105\n\t"
      "v_max_f32 v104, v104, v105\n\t"
      "v_sub_f32 v224, 0, v104\n\t"
      "v_sub_f32 v225, 0, v104\n\t"
      "v_sub_f32 v226, 0, v104\n\t"
      "v_sub_f32 v227, 0, v104\n\t"
      "v_sub_f32 v228, 0, v104\n\t"
      "v_sub_f32 v229, 0, v104\n\t"
      "v_sub_f32 v230, 0, v104\n\t"
      "v_sub_f32 v231, 0, v104\n\t"
      "v_sub_f32 v232, 0, v104\n\t"
      "v_sub_f32 v233, 0, v104\n\t"
      "v_sub_f32 v234, 0, v104\n\t"
      "v_sub_f32 v235, 0, v104\n\t"
      "v_sub_f32 v236, 0, v104\n\t"
      "v_sub_f32 v237, 0, v104\n\t"
      "v_sub_f32 v238, 0, v104\n\t"
      "v_sub_f32 v239, 0, v104\n\t"
      "v_mfma_f32_32x32x16_bf16 v[64:79], v[112:115], %[q7], 0\n\t"
      "s_nop 11\n\t"
      "v_max3_f32 v104, v64, v65, v66\n\t"
      "v_max3_f32 v105, v67, v68, v69\n\t"
      "v_max3_f32 v104, v104, v70, v71\n\t"
      "v_max3_f32 v105, v105, v72, v73\n\t"
      "v_max3_f32 v104, v104, v74, v75\n\t"
      "v_max3_f32 v105, v105, v76, v77\n\t"
      "v_max3_f32 v104, v104, v78, v79\n\t"
      "v_max_f32 v104, v104, v105\n\t"
      "v_mov_b32 v105, v104\n\t"
      "s_nop 1\n\t"
      "v_permlane32_swap_b32 v104, v105\n\t"
      "v_max_f32 v104, v104, v105\n\t"
      "v_sub_f32 v240, 0, v104\n\t"
      "v_sub_f32 v241, 0, v104\n\t"
      "v_sub_f32 v242, 0, v104\n\t"
      "v_sub_f32 v243, 0, v104\n\t"
      "v_sub_f32 v244, 0, v104\n\t"
      "v_sub_f32 v245, 0, v104\n\t"
      "v_sub_f32 v246, 0, v104\n\t"
      "v_sub_f32 v247, 0, v104\n\t"
      "v_sub_f32 v248, 0, v104\n\t"
      "v_sub_f32 v249, 0, v104\n\t"
      "v_sub_f32 v250, 0, v104\n\t"
      "v_sub_f32 v251, 0, v104\n\t"
      "v_sub_f32 v252, 0, v104\n\t"
      "v_sub_f32 v253, 0, v104\n\t"
      "v_sub_f32 v254, 0, v104\n\t"
      "v_sub_f32 v255, 0, v104\n\t"
      "s_mov_b32 s40, 32\n\t"
      "s_mov_b32 s41, 0x41000000\n\t"
      "v_mfma_f32_32x32x16_bf16 v[64:79], v[112:115], %[q0], v[128:143]\n\t"
      "s_waitcnt lgkmcnt(0)\n\t"
      "s_nop 7\n\t"
      "v_mov_b32 v100, 0\n\t"
      "v_mov_b32 v52, 0\n\t"
      "v_mov_b32 v101, 0\n\t"
      "v_mov_b32 v53, 0\n\t"
      "v_mov_b32 v102, 0\n\t"
      "v_mov_b32 v54, 0\n\t"
      "v_mov_b32 v103, 0\n\t"
      "v_mov_b32 v55, 0\n\t"
      "s_cmp_lg_u32 s43, 0\n\t"
      "s_cbranch_scc1 L_tops_%=\n\t"
      "L_topf_%=:\n\t"
      "v_exp_f32 v64, v64\n\t"
      "v_exp_f32 v65, v65\n\t"
      "v_mfma_f32_32x32x16_bf16 a[112:127], v[52:55], v[100:103], a[112:127]\n\t"
      "v_exp_f32 v66, v66\n\t"
      "v_exp_f32 v67, v67\n\t"
      "v_exp_f32 v68, v68\n\t"
      "v_mfma_f32_32x32x16_bf16 v[80:95], v[112:115], %[q1], v[144:159]\n\t"
      "v_exp_f32 v69, v69\n\t"
      "v_exp_f32 v70, v70\n\t"
      "v_exp_f32 v71, v71\n\t"
      "v_exp_f32 v72, v72\n\t"
      "v_cvt_pk_bf16_f32 v96, v64, v65\n\t"
      "v_cvt_pk_bf16_f32 v97, v66, v67\n\t"
      "v_cvt_pk_bf16_f32 v98, v68, v69\n\t"
      "v_cvt_pk_bf16_f32 v99, v70, v71\n\t"
      "v_exp_f32 v73, v73\n\t"
      "v_exp_f32 v74, v74\n\t"
      "v_mfma_f32_32x32x16_bf16 a[0:15], v[120:123], v[96:99], a[0:15]\n\t"
      "v_exp_f32 v75, v75\n\t"
      "v_exp_f32 v76, v76\n\t"
      "v_exp_f32 v77, v77\n\t"
      "v_exp_f32 v78, v78\n\t"
      "v_exp_f32 v79, v79\n\t"
      "v_cvt_pk_bf16_f32 v100, v72, v73\n\t"
      "v_cvt_pk_bf16_f32 v101, v74, v75\n\t"
      "v_cvt_pk_bf16_f32 v102, v76, v77\n\t"
      "v_cvt_pk_bf16_f32 v103, v78, v79\n\t"
      "v_exp_f32 v80, v80\n\t"
      "v_exp_f32 v81, v81\n\t"
      "v_mfma_f32_32x32x16_bf16 a[0:15], v[124:127], v[100:103], a[0:15]\n\t"
      "v_add_u32 %[vp], %[vp], %[vs]\n\t"
      "ds_read_b128 v[116:119], %[kp] offset:1024\n\t"
      "ds_read_b128 v[48:51], %[vp]\n\t"
      "ds_read_b128 v[52:55], %[vp] offset:512\n\t"
      "v_exp_f32 v82, v82\n\t"
      "v_exp_f32 v83, v83\n\t"
      "v_exp_f32 v84, v84\n\t"
      "v_mfma_f32_32x32x16_bf16 v[64:79], v[112:115], %[q2], v[160:175]\n\t"
      "v_exp_f32 v85, v85\n\t"
      "v_exp_f32 v86, v86\n\t"
      "v_exp_f32 v87, v87\n\t"
      "v_exp_f32 v88, v88\n\t"
      "v_cvt_pk_bf16_f32 v96, v80, v81\n\t"
      "v_cvt_pk_bf16_f32 v97, v82, v83\n\t"
      "v_cvt_pk_bf16_f32 v98, v84, v85\n\t"
      "v_cvt_pk_bf16_f32 v99, v86, v87\n\t"
      "v_exp_f32 v89, v89\n\t"
      "v_exp_f32 v90, v90\n\t"
      "v_mfma_f32_32x32x16_bf16 a[16:31], v[120:123], v[96:99], a[16:31]\n\t"
      "v_exp_f32 v91, v91\n\t"
      "v_exp_f32 v92, v92\n\t"
      "v_exp_f32 v93, v93\n\t"
      "v_exp_f32 v94, v94\n\t"
      "v_exp_f32 v95, v95\n\t"
      "v_cvt_pk_bf16_f32 v100, v88, v89\n\t"
      "v_cvt_pk_bf16_f32 v101, v90, v91\n\t"
      "v_cvt_pk_bf16_f32 v102, v92, v93\n\t"
      "v_cvt_pk_bf16_f32 v103, v94, v95\n\t"
      "v_exp_f32 v64, v64\n\t"
      "v_exp_f32 v65, v65\n\t"
      "v_mfma_f32_32x32x16_bf16 a[16:31], v[124:127], v[100:103], a[16:31]\n\t"
      "v_exp_f32 v66, v66\n\t"
      "v_exp_f32 v67, v67\n\t"
      "v_exp_f32 v68, v68\n\t"
      "v_mfma_f32_32x32x16_bf16 v[80:95], v[112:115], %[q3], v[176:191]\n\t"
      "v_exp_f32 v69, v69\n\t"
      "v_exp_f32 v70, v70\n\t"
      "v_exp_f32 v71, v71\n\t"
      "v_exp_f32 v72, v72\n\t"
      "v_cvt_pk_bf16_f32 v96, v64, v65\n\t"
      "v_cvt_pk_bf16_f32 v97, v66, v67\n\t"
      "v_cvt_pk_bf16_f32 v98, v68, v69\n\t"
      "v_cvt_pk_bf16_f32 v99, v70, v71\n\t"
      "v_exp_f32 v73, v73\n\t"
      "v_exp_f32 v74, v74\n\t"
      "v_mfma_f32_32x32x16_bf16 a[32:47], v[120:123], v[96:99], a[32:47]\n\t"
      "v_exp_f32 v75, v75\n\t"
      "v_exp_f32 v76, v76\n\t"
      "v_exp_f32 v77, v77\n\t"
      "v_exp_f32 v78, v78\n\t"
      "v_exp_f32 v79, v79\n\t"
      "v_cvt_pk_bf16_f32 v100, v72, v73\n\t"
      "v_cvt_pk_bf16_f32 v101, v74, v75\n\t"
      "v_cvt_pk_bf16_f32 v102, v76, v77\n\t"
      "v_cvt_pk_bf16_f32 v103, v78, v79\n\t"
      "v_exp_f32 v80, v80\n\t"
      "v_exp_f32 v81, v81\n\t"
      "v_mfma_f32_32x32x16_bf16 a[32:47], v[124:127], v[100:103], a[32:47]\n\t"
      "v_exp_f32 v82, v82\n\t"
      "v_exp_f32 v83, v83\n\t"
      "v_exp_f32 v84, v84\n\t"
      "v_mfma_f32_32x32x16_bf16 v[64:79], v[112:115], %[q4], v[192:207]\n\t"
      "v_exp_f32 v85, v85\n\t"
      "v_exp_f32 v86, v86\n\t"
      "v_exp_f32 v87, v87\n\t"
      "v_exp_f32 v88, v88\n\t"
      "v_cvt_pk_bf16_f32 v96, v80, v81\n\t"
      "v_cvt_pk_bf16_f32 v97, v82, v83\n\t"
      "v_cvt_pk_bf16_f32 v98, v84, v85\n\t"
      "v_cvt_pk_bf16_f32 v99, v86, v87\n\t"
      "v_exp_f32 v89, v89\n\t"
      "v_exp_f32 v90, v90\n\t"
      "v_mfma_f32_32x32x16_bf16 a[48:63], v[120:123], v[96:99], a[48:63]\n\t"
      "v_exp_f32 v91, v91\n\t"
      "v_exp_f32 v92, v92\n\t"
      "v_exp_f32 v93, v93\n\t"
      "v_exp_f32 v94, v94\n\t"
      "v_exp_f32 v95, v95\n\t"
      "v_cvt_pk_bf16_f32 v100, v88, v89\n\t"
      "v_cvt_pk_bf16_f32 v101, v90, v91\n\t"
      "v_cvt_pk_bf16_f32 v102, v92, v93\n\t"
      "v_cvt_pk_bf16_f32 v103, v94, v95\n\t"
      "v_exp_f32 v64, v64\n\t"
      "v_exp_f32 v65, v65\n\t"
      "v_mfma_f32_32x32x16_bf16 a[48:63], v[124:127], v[100:103], a[48:63]\n\t"
      "v_exp_f32 v66, v66\n\t"
      "v_exp_f32 v67, v67\n\t"
      "v_exp_f32 v68, v68\n\t"
      "v_mfma_f32_32x32x16_bf16 v[80:95], v[112:115], %[q5], v[208:223]\n\t"
      "v_exp_f32 v69, v69\n\t"
      "v_exp_f32 v70, v70\n\t"
      "v_exp_f32 v71, v71\n\t"
      "v_exp_f32 v72, v72\n\t"
      "v_cvt_pk_bf16_f32 v96, v64, v65\n\t"
      "v_cvt_pk_bf16_f32 v97, v66, v67\n\t"
      "v_cvt_pk_bf16_f32 v98, v68, v69\n\t"
      "v_cvt_pk_bf16_f32 v99, v70, v71\n\t"
      "v_exp_f32 v73, v73\n\t"
      "v_exp_f32 v74, v74\n\t"
      "v_mfma_f32_32x32x16_bf16 a[64:79], v[120:123], v[96:99], a[64:79]\n\t"
      "v_exp_f32 v75, v75\n\t"
      "v_exp_f32 v76, v76\n\t"
      "v_exp_f32 v77, v77\n\t"
      "v_exp_f32 v78, v78\n\t"
      "v_exp_f32 v79, v79\n\t"
      "v_cvt_pk_bf16_f32 v100, v72, v73\n\t"
      "v_cvt_pk_bf16_f32 v101, v74, v75\n\t"
      "v_cvt_pk_bf16_f32 v102, v76, v77\n\t"
      "v_cvt_pk_bf16_f32 v103, v78, v79\n\t"
      "v_exp_f32 v80, v80\n\t"
      "v_exp_f32 v81, v81\n\t"
      "v_mfma_f32_32x32x16_bf16 a[64:79], v[124:127], v[100:103], a[64:79]\n\t"
      "v_exp_f32 v82, v82\n\t"
      "v_exp_f32 v83, v83\n\t"
      "v_exp_f32 v84, v84\n\t"
      "v_mfma_f32_32x32x16_bf16 v[64:79], v[112:115], %[q6], v[224:239]\n\t"
      "v_exp_f32 v85, v85\n\t"
      "v_exp_f32 v86, v86\n\t"
      "v_exp_f32 v87, v87\n\t"
      "v_exp_f32 v88, v88\n\t"
      "v_cvt_pk_bf16_f32 v96, v80, v81\n\t"
      "v_cvt_pk_bf16_f32 v97, v82, v83\n\t"
      "v_cvt_pk_bf16_f32 v98, v84, v85\n\t"
      "v_cvt_pk_bf16_f32 v99, v86, v87\n\t"
      "v_exp_f32 v89, v89\n\t"
      "v_exp_f32 v90, v90\n\t"
      "v_mfma_f32_32x32x16_bf16 a[80:95], v[120:123], v[96:99], a[80:95]\n\t"
      "v_exp_f32 v91, v91\n\t"
      "v_exp_f32 v92, v92\n\t"
      "v_exp_f32 v93, v93\n\t"
      "v_exp_f32 v94, v94\n\t"
      "v_exp_f32 v95, v95\n\t"
      "v_cvt_pk_bf16_f32 v100, v88, v89\n\t"
      "v_cvt_pk_bf16_f32 v101, v90, v91\n\t"
      "v_cvt_pk_bf16_f32 v102, v92, v93\n\t"
      "v_cvt_pk_bf16_f32 v103, v94, v95\n\t"
      "v_exp_f32 v64, v64\n\t"
      "v_exp_f32 v65, v65\n\t"
      "v_mfma_f32_32x32x16_bf16 a[80:95], v[124:127], v[100:103], a[80:95]\n\t"
      "v_exp_f32 v66, v66\n\t"
      "v_exp_f32 v67, v67\n\t"
      "v_exp_f32 v68, v68\n\t"
      "v_mfma_f32_32x32x16_bf16 v[80:95], v[112:115], %[q7], v[240:255]\n\t"
      "v_exp_f32 v69, v69\n\t"
      "v_exp_f32 v70, v70\n\t"
      "v_exp_f32 v71, v71\n\t"
      "v_exp_f32 v72, v72\n\t"
      "v_cvt_pk_bf16_f32 v96, v64, v65\n\t"
      "v_cvt_pk_bf16_f32 v97, v66, v67\n\t"
      "v_cvt_pk_bf16_f32 v98, v68, v69\n\t"
      "v_cvt_pk_bf16_f32 v99, v70, v71\n\t"
      "v_exp_f32 v73, v73\n\t"
      "v_exp_f32 v74, v74\n\t"
      "v_mfma_f32_32x32x16_bf16 a[96:111], v[120:123], v[96:99], a[96:111]\n\t"
      "v_exp_f32 v75, v75\n\t"
      "v_exp_f32 v76, v76\n\t"
      "v_exp_f32 v77, v77\n\t"
      "v_exp_f32 v78, v78\n\t"
      "v_exp_f32 v79, v79\n\t"
      "v_cvt_pk_bf16_f32 v100, v72, v73\n\t"
      "v_cvt_pk_bf16_f32 v101, v74, v75\n\t"
      "v_cvt_pk_bf16_f32 v102, v76, v77\n\t"
      "v_cvt_pk_bf16_f32 v103, v78, v79\n\t"
      "v_exp_f32 v80, v80\n\t"
      "v_exp_f32 v81, v81\n\t"
      "v_mfma_f32_32x32x16_bf16 a[96:111], v[124:127], v[100:103], a[96:111]\n\t"
      "v_exp_f32 v82, v82\n\t"
      "v_exp_f32 v83, v83\n\t"
      "v_exp_f32 v84, v84\n\t"
      "s_waitcnt lgkmcnt(2)\n\t"
      "v_mfma_f32_32x32x16_bf16 v[64:79], v[116:119], %[q0], v[128:143]\n\t"
      "v_exp_f32 v85, v85\n\t"
      "v_exp_f32 v86, v86\n\t"
      "v_exp_f32 v87, v87\n\t"
      "v_exp_f32 v88, v88\n\t"
      "v_cvt_pk_bf16_f32 v96, v80, v81\n\t"
      "v_cvt_pk_bf16_f32 v97, v82, v83\n\t"
      "v_cvt_pk_bf16_f32 v98, v84, v85\n\t"
      "v_cvt_pk_bf16_f32 v99, v86, v87\n\t"
      "v_exp_f32 v89, v89\n\t"
      "v_exp_f32 v90, v90\n\t"
      "v_mfma_f32_32x32x16_bf16 a[112:127], v[120:123], v[96:99], a[112:127]\n\t"
      "v_exp_f32 v91, v91\n\t"
      "v_exp_f32 v92, v92\n\t"
      "v_exp_f32 v93, v93\n\t"
      "v_exp_f32 v94, v94\n\t"
      "v_exp_f32 v95, v95\n\t"
      "v_cvt_pk_bf16_f32 v100, v88, v89\n\t"
      "v_cvt_pk_bf16_f32 v101, v90, v91\n\t"
      "v_cvt_pk_bf16_f32 v102, v92, v93\n\t"
      "v_cvt_pk_bf16_f32 v103, v94, v95\n\t"
      "s_waitcnt lgkmcnt(0)\n\t"
      "v_exp_f32 v64, v64\n\t"
      "v_exp_f32 v65, v65\n\t"
      "v_mfma_f32_32x32x16_bf16 a[112:127], v[124:127], v[100:103], a[112:127]\n\t"
      "v_exp_f32 v66, v66\n\t"
      "v_exp_f32 v67, v67\n\t"
      "v_exp_f32 v68, v68\n\t"
      "v_mfma_f32_32x32x16_bf16 v[80:95], v[116:119], %[q1], v[144:159]\n\t"
      "v_exp_f32 v69, v69\n\t"
      "v_exp_f32 v70, v70\n\t"
      "v_exp_f32 v71, v71\n\t"
      "v_exp_f32 v72, v72\n\t"
      "v_cvt_pk_bf16_f32 v96, v64, v65\n\t"
      "v_cvt_pk_bf16_f32 v97, v66, v67\n\t"
      "v_cvt_pk_bf16_f32 v98, v68, v69\n\t"
      "v_cvt_pk_bf16_f32 v99, v70, v71\n\t"
      "v_exp_f32 v73, v73\n\t"
      "v_exp_f32 v74, v74\n\t"
      "v_mfma_f32_32x32x16_bf16 a[0:15], v[48:51], v[96:99], a[0:15]\n\t"
      "v_exp_f32 v75, v75\n\t"
      "v_exp_f32 v76, v76\n\t"
      "v_exp_f32 v77, v77\n\t"
      "v_exp_f32 v78, v78\n\t"
      "v_exp_f32 v79, v79\n\t"
      "v_cvt_pk_bf16_f32 v100, v72, v73\n\t"
      "v_cvt_pk_bf16_f32 v101, v74, v75\n\t"
      "v_cvt_pk_bf16_f32 v102, v76, v77\n\t"
      "v_cvt_pk_bf16_f32 v103, v78, v79\n\t"
      "v_exp_f32 v80, v80\n\t"
      "v_exp_f32 v81, v81\n\t"
      "v_mfma_f32_32x32x16_bf16 a[0:15], v[52:55], v[100:103], a[0:15]\n\t"
      "v_add_u32 %[vp], %[vp], %[vs]\n\t"
      "ds_read_b128 v[112:115], %[kp] offset:2048\n\t"
      "ds_read_b128 v[120:123], %[vp]\n\t"
      "ds_read_b128 v[124:127], %[vp] offset:512\n\t"
      "v_exp_f32 v82, v82\n\t"
      "v_exp_f32 v83, v83\n\t"
      "v_exp_f32 v84, v84\n\t"
      "v_mfma_f32_32x32x16_bf16 v[64:79], v[116:119], %[q2], v[160:175]\n\t"
      "v_exp_f32 v85, v85\n\t"
      "v_exp_f32 v86, v86\n\t"
      "v_exp_f32 v87, v87\n\t"
      "v_exp_f32 v88, v88\n\t"
      "v_cvt_pk_bf16_f32 v96, v80, v81\n\t"
      "v_cvt_pk_bf16_f32 v97, v82, v83\n\t"
      "v_cvt_pk_bf16_f32 v98, v84, v85\n\t"
      "v_cvt_pk_bf16_f32 v99, v86, v87\n\t"
      "v_exp_f32 v89, v89\n\t"
      "v_exp_f32 v90, v90\n\t"
      "v_mfma_f32_32x32x16_bf16 a[16:31], v[48:51], v[96:99], a[16:31]\n\t"
      "v_exp_f32 v91, v91\n\t"
      "v_exp_f32 v92, v92\n\t"
      "v_exp_f32 v93, v93\n\t"
      "v_exp_f32 v94, v94\n\t"
      "v_exp_f32 v95, v95\n\t"
      "v_cvt_pk_bf16_f32 v100, v88, v89\n\t"
      "v_cvt_pk_bf16_f32 v101, v90, v91\n\t"
      "v_cvt_pk_bf16_f32 v102, v92, v93\n\t"
      "v_cvt_pk_bf16_f32 v103, v94, v95\n\t"
      "v_exp_f32 v64, v64\n\t"
      "v_exp_f32 v65, v65\n\t"
      "v_mfma_f32_32x32x16_bf16 a[16:31], v[52:55], v[100:103], a[16:31]\n\t"
      "v_exp_f32 v66, v66\n\t"
      "v_exp_f32 v67, v67\n\t"
      "v_exp_f32 v68, v68\n\t"
      "v_mfma_f32_32x32x16_bf16 v[80:95], v[116:119], %[q3], v[176:191]\n\t"
      "v_exp_f32 v69, v69\n\t"
      "v_exp_f32 v70, v70\n\t"
      "v_exp_f32 v71, v71\n\t"
      "v_exp_f32 v72, v72\n\t"
      "v_cvt_pk_bf16_f32 v96, v64, v65\n\t"
      "v_cvt_pk_bf16_f32 v97, v66, v67\n\t"
      "v_cvt_pk_bf16_f32 v98, v68, v69\n\t"
      "v_cvt_pk_bf16_f32 v99, v70, v71\n\t"
      "v_exp_f32 v73, v73\n\t"
      "v_exp_f32 v74, v74\n\t"
      "v_mfma_f32_32x32x16_bf16 a[32:47], v[48:51], v[96:99], a[32:47]\n\t"
      "v_exp_f32 v75, v75\n\t"
      "v_exp_f32 v76, v76\n\t"
      "v_exp_f32 v77, v77\n\t"
      "v_exp_f32 v78, v78\n\t"
      "v_exp_f32 v79, v79\n\t"
      "v_cvt_pk_bf16_f32 v100, v72, v73\n\t"
      "v_cvt_pk_bf16_f32 v101, v74, v75\n\t"
      "v_cvt_pk_bf16_f32 v102, v76, v77\n\t"
      "v_cvt_pk_bf16_f32 v103, v78, v79\n\t"
      "v_exp_f32 v80, v80\n\t"
      "v_exp_f32 v81, v81\n\t"
      "v_mfma_f32_32x32x16_bf16 a[32:47], v[52:55], v[100:103], a[32:47]\n\t"
      "v_exp_f32 v82, v82\n\t"
      "v_exp_f32 v83, v83\n\t"
      "v_exp_f32 v84, v84\n\t"
      "v_mfma_f32_32x32x16_bf16 v[64:79], v[116:119], %[q4], v[192:207]\n\t"
      "v_exp_f32 v85, v85\n\t"
      "v_exp_f32 v86, v86\n\t"
      "v_exp_f32 v87, v87\n\t"
      "v_exp_f32 v88, v88\n\t"
      "v_cvt_pk_bf16_f32 v96, v80, v81\n\t"
      "v_cvt_pk_bf16_f32 v97, v82, v83\n\t"
      "v_cvt_pk_bf16_f32 v98, v84, v85\n\t"
      "v_cvt_pk_bf16_f32 v99, v86, v87\n\t"
      "v_exp_f32 v89, v89\n\t"
      "v_exp_f32 v90, v90\n\t"
      "v_mfma_f32_32x32x16_bf16 a[48:63], v[48:51], v[96:99], a[48:63]\n\t"
      "v_exp_f32 v91, v91\n\t"
      "v_exp_f32 v92, v92\n\t"
      "v_exp_f32 v93, v93\n\t"
      "v_exp_f32 v94, v94\n\t"
      "v_exp_f32 v95, v95\n\t"
      "v_cvt_pk_bf16_f32 v100, v88, v89\n\t"
      "v_cvt_pk_bf16_f32 v101, v90, v91\n\t"
      "v_cvt_pk_bf16_f32 v102, v92, v93\n\t"
      "v_cvt_pk_bf16_f32 v103, v94, v95\n\t"
      "v_exp_f32 v64, v64\n\t"
      "v_exp_f32 v65, v65\n\t"
      "v_mfma_f32_32x32x16_bf16 a[48:63], v[52:55], v[100:103], a[48:63]\n\t"
      "v_exp_f32 v66, v66\n\t"
      "v_exp_f32 v67, v67\n\t"
      "v_exp_f32 v68, v68\n\t"
      "v_mfma_f32_32x32x16_bf16 v[80:95], v[116:119], %[q5], v[208:223]\n\t"
      "v_exp_f32 v69, v69\n\t"
      "v_exp_f32 v70, v70\n\t"
      "v_exp_f32 v71, v71\n\t"
      "v_exp_f32 v72, v72\n\t"
      "v_cvt_pk_bf16_f32 v96, v64, v65\n\t"
      "v_cvt_pk_bf16_f32 v97, v66, v67\n\t"
      "v_cvt_pk_bf16_f32 v98, v68, v69\n\t"
      "v_cvt_pk_bf16_f32 v99, v70, v71\n\t"
      "v_exp_f32 v73, v73\n\t"
      "v_exp_f32 v74, v74\n\t"
      "v_mfma_f32_32x32x16_bf16 a[64:79], v[48:51], v[96:99], a[64:79]\n\t"
      "v_exp_f32 v75, v75\n\t"
      "v_exp_f32 v76, v76\n\t"
      "v_exp_f32 v77, v77\n\t"
      "v_exp_f32 v78, v78\n\t"
      "v_exp_f32 v79, v79\n\t"
      "v_cvt_pk_bf16_f32 v100, v72, v73\n\t"
      "v_cvt_pk_bf16_f32 v101, v74, v75\n\t"
      "v_cvt_pk_bf16_f32 v102, v76, v77\n\t"
      "v_cvt_pk_bf16_f32 v103, v78, v79\n\t"
      "v_exp_f32 v80, v80\n\t"
      "v_exp_f32 v81, v81\n\t"
      "v_mfma_f32_32x32x16_bf16 a[64:79], v[52:55], v[100:103], a[64:79]\n\t"
      "v_exp_f32 v82, v82\n\t"
      "v_exp_f32 v83, v83\n\t"
      "v_exp_f32 v84, v84\n\t"
      "v_mfma_f32_32x32x16_bf16 v[64:79], v[116:119], %[q6], v[224:239]\n\t"
      "v_exp_f32 v85, v85\n\t"
      "v_exp_f32 v86, v86\n\t"
      "v_exp_f32 v87, v87\n\t"
      "v_exp_f32 v88, v88\n\t"
      "v_cvt_pk_bf16_f32 v96, v80, v81\n\t"
      "v_cvt_pk_bf16_f32 v97, v82, v83\n\t"
      "v_cvt_pk_bf16_f32 v98, v84, v85\n\t"
      "v_cvt_pk_bf16_f32 v99, v86, v87\n\t"
      "v_exp_f32 v89, v89\n\t"
      "v_exp_f32 v90, v90\n\t"
      "v_mfma_f32_32x32x16_bf16 a[80:95], v[48:51], v[96:99], a[80:95]\n\t"
      "v_exp_f32 v91, v91\n\t"
      "v_exp_f32 v92, v92\n\t"
      "v_exp_f32 v93, v93\n\t"
      "v_exp_f32 v94, v94\n\t"
      "v_exp_f32 v95, v95\n\t"
      "v_cvt_pk_bf16_f32 v100, v88, v89\n\t"
      "v_cvt_pk_bf16_f32 v101, v90, v91\n\t"
      "v_cvt_pk_bf16_f32 v102, v92, v93\n\t"
      "v_cvt_pk_bf16_f32 v103, v94, v95\n\t"
      "v_exp_f32 v64, v64\n\t"
      "v_exp_f32 v65, v65\n\t"
      "v_mfma_f32_32x32x16_bf16 a[80:95], v[52:55], v[100:103], a[80:95]\n\t"
      "v_exp_f32 v66, v66\n\t"
      "v_exp_f32 v67, v67\n\t"
      "v_exp_f32 v68, v68\n\t"
      "v_mfma_f32_32x32x16_bf16 v[80:95], v[116:119], %[q7], v[240:255]\n\t"
      "v_exp_f32 v69, v69\n\t"
      "v_exp_f32 v70, v70\n\t"
      "v_exp_f32 v71, v71\n\t"
      "v_exp_f32 v72, v72\n\t"
      "v_cvt_pk_bf16_f32 v96, v64, v65\n\t"
      "v_cvt_pk_bf16_f32 v97, v66, v67\n\t"
      "v_cvt_pk_bf16_f32 v98, v68, v69\n\t"
      "v_cvt_pk_bf16_f32 v99, v70, v71\n\t"
      "v_exp_f32 v73, v73\n\t"
      "v_exp_f32 v74, v74\n\t"
      "v_mfma_f32_32x32x16_bf16 a[96:111], v[48:51], v[96:99], a[96:111]\n\t"
      "v_exp_f32 v75, v75\n\t"
      "v_exp_f32 v76, v76\n\t"
      "v_exp_f32 v77, v77\n\t"
      "v_exp_f32 v78, v78\n\t"
      "v_exp_f32 v79, v79\n\t"
      "v_cvt_pk_bf16_f32 v100, v72, v73\n\t"
      "v_cvt_pk_bf16_f32 v101, v74, v75\n\t"
      "v_cvt_pk_bf16_f32 v102, v76, v77\n\t"
      "v_cvt_pk_bf16_f32 v103, v78, v79\n\t"
      "v_exp_f32 v80, v80\n\t"
      "v_exp_f32 v81, v81\n\t"
      "v_mfma_f32_32x32x16_bf16 a[96:111], v[52:55], v[100:103], a[96:111]\n\t"
      "v_exp_f32 v82, v82\n\t"
      "v_exp_f32 v83, v83\n\t"
      "v_exp_f32 v84, v84\n\t"
      "s_waitcnt lgkmcnt(2)\n\t"
      "v_mfma_f32_32x32x16_bf16 v[64:79], v[112:115], %[q0], v[128:143]\n\t"
      "v_exp_f32 v85, v85\n\t"
      "v_exp_f32 v86, v86\n\t"
      "v_exp_f32 v87, v87\n\t"
      "v_exp_f32 v88, v88\n\t"
      "v_cvt_pk_bf16_f32 v96, v80, v81\n\t"
      "v_cvt_pk_bf16_f32 v97, v82, v83\n\t"
      "v_cvt_pk_bf16_f32 v98, v84, v85\n\t"
      "v_cvt_pk_bf16_f32 v99, v86, v87\n\t"
      "v_exp_f32 v89, v89\n\t"
      "v_exp_f32 v90, v90\n\t"
      "v_mfma_f32_32x32x16_bf16 a[112:127], v[48:51], v[96:99], a[112:127]\n\t"
      "v_exp_f32 v91, v91\n\t"
      "v_exp_f32 v92, v92\n\t"
      "v_exp_f32 v93, v93\n\t"
      "v_exp_f32 v94, v94\n\t"
      "v_exp_f32 v95, v95\n\t"
      "v_cvt_pk_bf16_f32 v100, v88, v89\n\t"
      "v_cvt_pk_bf16_f32 v101, v90, v91\n\t"
      "v_cvt_pk_bf16_f32 v102, v92, v93\n\t"
      "v_cvt_pk_bf16_f32 v103, v94, v95\n\t"
      "s_waitcnt lgkmcnt(0)\n\t"
      "v_add_u32 %[kp], 0x800, %[kp]\n\t"
      "s_sub_u32 s40, s40, 1\n\t"
      "s_cmp_lg_u32 s40, 0\n\t"
      "s_cbranch_scc1 L_topf_%=\n\t"
      "v_mfma_f32_32x32x16_bf16 a[112:127], v[52:55], v[100:103], a[112:127]\n\t"
      "s_nop 15\n\t"
      "s_mov_b32 s42, 0x71800000\n\t"
      "s_mov_b64 s[44:45], 0\n\t"
      "v_accvgpr_read_b32 v104, a8\n\t"
      "v_cmp_ngt_f32 vcc, s42, v104\n\t"
      "s_or_b64 s[44:45], s[44:45], vcc\n\t"
      "v_accvgpr_read_b32 v104, a24\n\t"
      "v_cmp_ngt_f32 vcc, s42, v104\n\t"
      "s_or_b64 s[44:45], s[44:45], vcc\n\t"
      "v_accvgpr_read_b32 v104, a40\n\t"
      "v_cmp_ngt_f32 vcc, s42, v104\n\t"
      "s_or_b64 s[44:45], s[44:45], vcc\n\t"
      "v_accvgpr_read_b32 v104, a56\n\t"
      "v_cmp_ngt_f32 vcc, s42, v104\n\t"
      "s_or_b64 s[44:45], s[44:45], vcc\n\t"
      "v_accvgpr_read_b32 v104, a72\n\t"
      "v_cmp_ngt_f32 vcc, s42, v104\n\t"
      "s_or_b64 s[44:45], s[44:45], vcc\n\t"
      "v_accvgpr_read_b32 v104, a88\n\t"
      "v_cmp_ngt_f32 vcc, s42, v104\n\t"
      "s_or_b64 s[44:45], s[44:45], vcc\n\t"
      "v_accvgpr_read_b32 v104, a104\n\t"
      "v_cmp_ngt_f32 vcc, s42, v104\n\t"
      "s_or_b64 s[44:45], s[44:45], vcc\n\t"
      "v_accvgpr_read_b32 v104, a120\n\t"
      "v_cmp_ngt_f32 vcc, s42, v104\n\t"
      "s_or_b64 s[44:45], s[44:45], vcc\n\t"
      "s_cmp_eq_u64 s[44:45], 0\n\t"
      "s_cbranch_scc1 L_fin_%=\n\t"
      "v_mov_b32 %[kp], v107\n\t"
      "v_mov_b32 %[vp], v108\n\t"
      "s_mov_b32 s43, 1\n\t"
      "s_branch L_start_%=\n\t"
      "L_tops_%=:\n\t"
      "v_max3_f32 v104, v64, v65, v66\n\t"
      "v_max3_f32 v105, v67, v68, v69\n\t"
      "v_mfma_f32_32x32x16_bf16 a[112:127], v[52:55], v[100:103], a[112:127]\n\t"
      "v_max3_f32 v104, v104, v70, v71\n\t"
      "v_max3_f32 v105, v105, v72, v73\n\t"
      "v_max3_f32 v104, v104, v74, v75\n\t"
      "v_max3_f32 v105, v105, v76, v77\n\t"
      "v_max3_f32 v104, v104, v78, v79\n\t"
      "v_max_f32 v104, v104, v105\n\t"
      "v_cmp_lt_f32 vcc, s41, v104\n\t"
      "v_mfma_f32_32x32x16_bf16 v[80:95], v[112:115], %[q1], v[144:159]\n\t"
      "s_cbranch_vccnz L_rare00_%=\n\t"
      "L_back00_%=:\n\t"
      "v_exp_f32 v64, v64\n\t"
      "v_exp_f32 v65, v65\n\t"
      "v_exp_f32 v66, v66\n\t"
      "v_exp_f32 v67, v67\n\t"
      "v_exp_f32 v68, v68\n\t"
      "v_exp_f32 v69, v69\n\t"
      "v_exp_f32 v70, v70\n\t"
      "v_exp_f32 v71, v71\n\t"
      "v_exp_f32 v72, v72\n\t"
      "v_cvt_pk_bf16_f32 v96, v64, v65\n\t"
      "v_cvt_pk_bf16_f32 v97, v66, v67\n\t"
      "v_cvt_pk_bf16_f32 v98, v68, v69\n\t"
      "v_cvt_pk_bf16_f32 v99, v70, v71\n\t"
      "v_exp_f32 v73, v73\n\t"
      "v_exp_f32 v74, v74\n\t"
      "v_mfma_f32_32x32x16_bf16 a[0:15], v[120:123], v[96:99], a[0:15]\n\t"
      "v_exp_f32 v75, v75\n\t"
      "v_exp_f32 v76, v76\n\t"
      "v_exp_f32 v77, v77\n\t"
      "v_exp_f32 v78, v78\n\t"
      "v_exp_f32 v79, v79\n\t"
      "v_cvt_pk_bf16_f32 v100, v72, v73\n\t"
      "v_cvt_pk_bf16_f32 v101, v74, v75\n\t"
      "v_cvt_pk_bf16_f32 v102, v76, v77\n\t"
      "v_cvt_pk_bf16_f32 v103, v78, v79\n\t"
      "v_max3_f32 v104, v80, v81, v82\n\t"
      "v_max3_f32 v105, v83, v84, v85\n\t"
      "v_mfma_f32_32x32x16_bf16 a[0:15], v[124:127], v[100:103], a[0:15]\n\t"
      "v_add_u32 %[vp], %[vp], %[vs]\n\t"
      "ds_read_b128 v[116:119], %[kp] offset:1024\n\t"
      "ds_read_b128 v[48:51], %[vp]\n\t"
      "ds_read_b128 v[52:55], %[vp] offset:512\n\t"
      "v_max3_f32 v104, v104, v86, v87\n\t"
      "v_max3_f32 v105, v105, v88, v89\n\t"
      "v_max3_f32 v104, v104, v90, v91\n\t"
      "v_max3_f32 v105, v105, v92, v93\n\t"
      "v_max3_f32 v104, v104, v94, v95\n\t"
      "v_max_f32 v104, v104, v105\n\t"
      "v_cmp_lt_f32 vcc, s41, v104\n\t"
      "v_mfma_f32_32x32x16_bf16 v[64:79], v[112:115], %[q2], v[160:175]\n\t"
      "s_cbranch_vccnz L_rare01_%=\n\t"
      "L_back01_%=:\n\t"
      "v_exp_f32 v80, v80\n\t"
      "v_exp_f32 v81, v81\n\t"
      "v_exp_f32 v82, v82\n\t"
      "v_exp_f32 v83, v83\n\t"
      "v_exp_f32 v84, v84\n\t"
      "v_exp_f32 v85, v85\n\t"
      "v_exp_f32 v86, v86\n\t"
      "v_exp_f32 v87, v87\n\t"
      "v_exp_f32 v88, v88\n\t"
      "v_cvt_pk_bf16_f32 v96, v80, v81\n\t"
      "v_cvt_pk_bf16_f32 v97, v82, v83\n\t"
      "v_cvt_pk_bf16_f32 v98, v84, v85\n\t"
      "v_cvt_pk_bf16_f32 v99, v86, v87\n\t"
      "v_exp_f32 v89, v89\n\t"
      "v_exp_f32 v90, v90\n\t"
      "v_mfma_f32_32x32x16_bf16 a[16:31], v[120:123], v[96:99], a[16:31]\n\t"
      "v_exp_f32 v91, v91\n\t"
      "v_exp_f32 v92, v92\n\t"
      "v_exp_f32 v93, v93\n\t"
      "v_exp_f32 v94, v94\n\t"
      "v_exp_f32 v95, v95\n\t"
      "v_cvt_pk_bf16_f32 v100, v88, v89\n\t"
      "v_cvt_pk_bf16_f32 v101, v90, v91\n\t"
      "v_cvt_pk_bf16_f32 v102, v92, v93\n\t"
      "v_cvt_pk_bf16_f32 v103, v94, v95\n\t"
      "v_max3_f32 v104, v64, v65, v66\n\t"
      "v_max3_f32 v105, v67, v68, v69\n\t"
      "v_mfma_f32_32x32x16_bf16 a[16:31], v[124:127], v[100:103], a[16:31]\n\t"
      "v_max3_f32 v104, v104, v70, v71\n\t"
      "v_max3_f32 v105, v105, v72, v73\n\t"
      "v_max3_f32 v104, v104, v74, v75\n\t"
      "v_max3_f32 v105, v105, v76, v77\n\t"
      "v_max3_f32 v104, v104, v78, v79\n\t"
      "v_max_f32 v104, v104, v105\n\t"
      "v_cmp_lt_f32 vcc, s41, v104\n\t"
      "v_mfma_f32_32x32x16_bf16 v[80:95], v[112:115], %[q3], v[176:191]\n\t"
      "s_cbranch_vccnz L_rare02_%=\n\t"
      "L_back02_%=:\n\t"
      "v_exp_f32 v64, v64\n\t"
      "v_exp_f32 v65, v65\n\t"
      "v_exp_f32 v66, v66\n\t"
      "v_exp_f32 v67, v67\n\t"
      "v_exp_f32 v68, v68\n\t"
      "v_exp_f32 v69, v69\n\t"
      "v_exp_f32 v70, v70\n\t"
      "v_exp_f32 v71, v71\n\t"
      "v_exp_f32 v72, v72\n\t"
      "v_cvt_pk_bf16_f32 v96, v64, v65\n\t"
      "v_cvt_pk_bf16_f32 v97, v66, v67\n\t"
      "v_cvt_pk_bf16_f32 v98, v68, v69\n\t"
      "v_cvt_pk_bf16_f32 v99, v70, v71\n\t"
      "v_exp_f32 v73, v73\n\t"
      "v_exp_f32 v74, v74\n\t"
      "v_mfma_f32_32x32x16_bf16 a[32:47], v[120:123], v[96:99], a[32:47]\n\t"
      "v_exp_f32 v75, v75\n\t"
      "v_exp_f32 v76, v76\n\t"
      "v_exp_f32 v77, v77\n\t"
      "v_exp_f32 v78, v78\n\t"
      "v_exp_f32 v79, v79\n\t"
      "v_cvt_pk_bf16_f32 v100, v72, v73\n\t"
      "v_cvt_pk_bf16_f32 v101, v74, v75\n\t"
      "v_cvt_pk_bf16_f32 v102, v76, v77\n\t"
      "v_cvt_pk_bf16_f32 v103, v78, v79\n\t"
      "v_max3_f32 v104, v80, v81, v82\n\t"
      "v_max3_f32 v105, v83, v84, v85\n\t"
      "v_mfma_f32_32x32x16_bf16 a[32:47], v[124:127], v[100:103], a[32:47]\n\t"
      "v_max3_f32 v104, v104, v86, v87\n\t"
      "v_max3_f32 v105, v105, v88, v89\n\t"
      "v_max3_f32 v104, v104, v90, v91\n\t"
      "v_max3_f32 v105, v105, v92, v93\n\t"
      "v_max3_f32 v104, v104, v94, v95\n\t"
      "v_max_f32 v104, v104, v105\n\t"
      "v_cmp_lt_f32 vcc, s41, v104\n\t"
      "v_mfma_f32_32x32x16_bf16 v[64:79], v[112:115], %[q4], v[192:207]\n\t"
      "s_cbranch_vccnz L_rare03_%=\n\t"
      "L_back03_%=:\n\t"
      "v_exp_f32 v80, v80\n\t"
      "v_exp_f32 v81, v81\n\t"
      "v_exp_f32 v82, v82\n\t"
      "v_exp_f32 v83, v83\n\t"
      "v_exp_f32 v84, v84\n\t"
      "v_exp_f32 v85, v85\n\t"
      "v_exp_f32 v86, v86\n\t"
      "v_exp_f32 v87, v87\n\t"
      "v_exp_f32 v88, v88\n\t"
      "v_cvt_pk_bf16_f32 v96, v80, v81\n\t"
      "v_cvt_pk_bf16_f32 v97, v82, v83\n\t"
      "v_cvt_pk_bf16_f32 v98, v84, v85\n\t"
      "v_cvt_pk_bf16_f32 v99, v86, v87\n\t"
      "v_exp_f32 v89, v89\n\t"
      "v_exp_f32 v90, v90\n\t"
      "v_mfma_f32_32x32x16_bf16 a[48:63], v[120:123], v[96:99], a[48:63]\n\t"
      "v_exp_f32 v91, v91\n\t"
      "v_exp_f32 v92, v92\n\t"
      "v_exp_f32 v93, v93\n\t"
      "v_exp_f32 v94, v94\n\t"
      "v_exp_f32 v95, v95\n\t"
      "v_cvt_pk_bf16_f32 v100, v88, v89\n\t"
      "v_cvt_pk_bf16_f32 v101, v90, v91\n\t"
      "v_cvt_pk_bf16_f32 v102, v92, v93\n\t"
      "v_cvt_pk_bf16_f32 v103, v94, v95\n\t"
      "v_max3_f32 v104, v64, v65, v66\n\t"
      "v_max3_f32 v105, v67, v68, v69\n\t"
      "v_mfma_f32_32x32x16_bf16 a[48:63], v[124:127], v[100:103], a[48:63]\n\t"
      "v_max3_f32 v104, v104, v70, v71\n\t"
      "v_max3_f32 v105, v105, v72, v73\n\t"
      "v_max3_f32 v104, v104, v74, v75\n\t"
      "v_max3_f32 v105, v105, v76, v77\n\t"
      "v_max3_f32 v104, v104, v78, v79\n\t"
      "v_max_f32 v104, v104, v105\n\t"
      "v_cmp_lt_f32 vcc, s41, v104\n\t"
      "v_mfma_f32_32x32x16_bf16 v[80:95], v[112:115], %[q5], v[208:223]\n\t"
      "s_cbranch_vccnz L_rare04_%=\n\t"
      "L_back04_%=:\n\t"
      "v_exp_f32 v64, v64\n\t"
      "v_exp_f32 v65, v65\n\t"
      "v_exp_f32 v66, v66\n\t"
      "v_exp_f32 v67, v67\n\t"
      "v_exp_f32 v68, v68\n\t"
      "v_exp_f32 v69, v69\n\t"
      "v_exp_f32 v70, v70\n\t"
      "v_exp_f32 v71, v71\n\t"
      "v_exp_f32 v72, v72\n\t"
      "v_cvt_pk_bf16_f32 v96, v64, v65\n\t"
      "v_cvt_pk_bf16_f32 v97, v66, v67\n\t"
      "v_cvt_pk_bf16_f32 v98, v68, v69\n\t"
      "v_cvt_pk_bf16_f32 v99, v70, v71\n\t"
      "v_exp_f32 v73, v73\n\t"
      "v_exp_f32 v74, v74\n\t"
      "v_mfma_f32_32x32x16_bf16 a[64:79], v[120:123], v[96:99], a[64:79]\n\t"
      "v_exp_f32 v75, v75\n\t"
      "v_exp_f32 v76, v76\n\t"
      "v_exp_f32 v77, v77\n\t"
      "v_exp_f32 v78, v78\n\t"
      "v_exp_f32 v79, v79\n\t"
      "v_cvt_pk_bf16_f32 v100, v72, v73\n\t"
      "v_cvt_pk_bf16_f32 v101, v74, v75\n\t"
      "v_cvt_pk_bf16_f32 v102, v76, v77\n\t"
      "v_cvt_pk_bf16_f32 v103, v78, v79\n\t"
      "v_max3_f32 v104, v80, v81, v82\n\t"
      "v_max3_f32 v105, v83, v84, v85\n\t"
      "v_mfma_f32_32x32x16_bf16 a[64:79], v[124:127], v[100:103], a[64:79]\n\t"
      "v_max3_f32 v104, v104, v86, v87\n\t"
      "v_max3_f32 v105, v105, v88, v89\n\t"
      "v_max3_f32 v104, v104, v90, v91\n\t"
      "v_max3_f32 v105, v105, v92, v93\n\t"
      "v_max3_f32 v104, v104, v94, v95\n\t"
      "v_max_f32 v104, v104, v105\n\t"
      "v_cmp_lt_f32 vcc, s41, v104\n\t"
      "v_mfma_f32_32x32x16_bf16 v[64:79], v[112:115], %[q6], v[224:239]\n\t"
      "s_cbranch_vccnz L_rare05_%=\n\t"
      "L_back05_%=:\n\t"
      "v_exp_f32 v80, v80\n\t"
      "v_exp_f32 v81, v81\n\t"
      "v_exp_f32 v82, v82\n\t"
      "v_exp_f32 v83, v83\n\t"
      "v_exp_f32 v84, v84\n\t"
      "v_exp_f32 v85, v85\n\t"
      "v_exp_f32 v86, v86\n\t"
      "v_exp_f32 v87, v87\n\t"
      "v_exp_f32 v88, v88\n\t"
      "v_cvt_pk_bf16_f32 v96, v80, v81\n\t"
      "v_cvt_pk_bf16_f32 v97, v82, v83\n\t"
      "v_cvt_pk_bf16_f32 v98, v84, v85\n\t"
      "v_cvt_pk_bf16_f32 v99, v86, v87\n\t"
      "v_exp_f32 v89, v89\n\t"
      "v_exp_f32 v90, v90\n\t"
      "v_mfma_f32_32x32x16_bf16 a[80:95], v[120:123], v[96:99], a[80:95]\n\t"
      "v_exp_f32 v91, v91\n\t"
      "v_exp_f32 v92, v92\n\t"
      "v_exp_f32 v93, v93\n\t"
      "v_exp_f32 v94, v94\n\t"
      "v_exp_f32 v95, v95\n\t"
      "v_cvt_pk_bf16_f32 v100, v88, v89\n\t"
      "v_cvt_pk_bf16_f32 v101, v90, v91\n\t"
      "v_cvt_pk_bf16_f32 v102, v92, v93\n\t"
      "v_cvt_pk_bf16_f32 v103, v94, v95\n\t"
      "v_max3_f32 v104, v64, v65, v66\n\t"
      "v_max3_f32 v105, v67, v68, v69\n\t"
      "v_mfma_f32_32x32x16_bf16 a[80:95], v[124:127], v[100:103], a[80:95]\n\t"
      "v_max3_f32 v104, v104, v70, v71\n\t"
      "v_max3_f32 v105, v105, v72, v73\n\t"
      "v_max3_f32 v104, v104, v74, v75\n\t"
      "v_max3_f32 v105, v105, v76, v77\n\t"
      "v_max3_f32 v104, v104, v78, v79\n\t"
      "v_max_f32 v104, v104, v105\n\t"
      "v_cmp_lt_f32 vcc, s41, v104\n\t"
      "v_mfma_f32_32x32x16_bf16 v[80:95], v[112:115], %[q7], v[240:255]\n\t"
      "s_cbranch_vccnz L_rare06_%=\n\t"
      "L_back06_%=:\n\t"
      "v_exp_f32 v64, v64\n\t"
      "v_exp_f32 v65, v65\n\t"
      "v_exp_f32 v66, v66\n\t"
      "v_exp_f32 v67, v67\n\t"
      "v_exp_f32 v68, v68\n\t"
      "v_exp_f32 v69, v69\n\t"
      "v_exp_f32 v70, v70\n\t"
      "v_exp_f32 v71, v71\n\t"
      "v_exp_f32 v72, v72\n\t"
      "v_cvt_pk_bf16_f32 v96, v64, v65\n\t"
      "v_cvt_pk_bf16_f32 v97, v66, v67\n\t"
      "v_cvt_pk_bf16_f32 v98, v68, v69\n\t"
      "v_cvt_pk_bf16_f32 v99, v70, v71\n\t"
      "v_exp_f32 v73, v73\n\t"
      "v_exp_f32 v74, v74\n\t"
      "v_mfma_f32_32x32x16_bf16 a[96:111], v[120:123], v[96:99], a[96:111]\n\t"
      "v_exp_f32 v75, v75\n\t"
      "v_exp_f32 v76, v76\n\t"
      "v_exp_f32 v77, v77\n\t"
      "v_exp_f32 v78, v78\n\t"
      "v_exp_f32 v79, v79\n\t"
      "v_cvt_pk_bf16_f32 v100, v72, v73\n\t"
      "v_cvt_pk_bf16_f32 v101, v74, v75\n\t"
      "v_cvt_pk_bf16_f32 v102, v76, v77\n\t"
      "v_cvt_pk_bf16_f32 v103, v78, v79\n\t"
      "v_max3_f32 v104, v80, v81, v82\n\t"
      "v_max3_f32 v105, v83, v84, v85\n\t"
      "v_mfma_f32_32x32x16_bf16 a[96:111], v[124:127], v[100:103], a[96:111]\n\t"
      "v_max3_f32 v104, v104, v86, v87\n\t"
      "v_max3_f32 v105, v105, v88, v89\n\t"
      "v_max3_f32 v104, v104, v90, v91\n\t"
      "v_max3_f32 v105, v105, v92, v93\n\t"
      "v_max3_f32 v104, v104, v94, v95\n\t"
      "v_max_f32 v104, v104, v105\n\t"
      "v_cmp_lt_f32 vcc, s41, v104\n\t"
      "s_waitcnt lgkmcnt(2)\n\t"
      "v_mfma_f32_32x32x16_bf16 v[64:79], v[116:119], %[q0], v[128:143]\n\t"
      "s_cbranch_vccnz L_rare07_%=\n\t"
      "L_back07_%=:\n\t"
      "v_exp_f32 v80, v80\n\t"
      "v_exp_f32 v81, v81\n\t"
      "v_exp_f32 v82, v82\n\t"
      "v_exp_f32 v83, v83\n\t"
      "v_exp_f32 v84, v84\n\t"
      "v_exp_f32 v85, v85\n\t"
      "v_exp_f32 v86, v86\n\t"
      "v_exp_f32 v87, v87\n\t"
      "v_exp_f32 v88, v88\n\t"
      "v_cvt_pk_bf16_f32 v96, v80, v81\n\t"
      "v_cvt_pk_bf16_f32 v97, v82, v83\n\t"
      "v_cvt_pk_bf16_f32 v98, v84, v85\n\t"
      "v_cvt_pk_bf16_f32 v99, v86, v87\n\t"
      "v_exp_f32 v89, v89\n\t"
      "v_exp_f32 v90, v90\n\t"
      "v_mfma_f32_32x32x16_bf16 a[112:127], v[120:123], v[96:99], a[112:127]\n\t"
      "v_exp_f32 v91, v91\n\t"
      "v_exp_f32 v92, v92\n\t"
      "v_exp_f32 v93, v93\n\t"
      "v_exp_f32 v94, v94\n\t"
      "v_exp_f32 v95, v95\n\t"
      "v_cvt_pk_bf16_f32 v100, v88, v89\n\t"
      "v_cvt_pk_bf16_f32 v101, v90, v91\n\t"
      "v_cvt_pk_bf16_f32 v102, v92, v93\n\t"
      "v_cvt_pk_bf16_f32 v103, v94, v95\n\t"
      "s_waitcnt lgkmcnt(0)\n\t"
      "v_max3_f32 v104, v64, v65, v66\n\t"
      "v_max3_f32 v105, v67, v68, v69\n\t"
      "v_mfma_f32_32x32x16_bf16 a[112:127], v[124:127], v[100:103], a[112:127]\n\t"
      "v_max3_f32 v104, v104, v70, v71\n\t"
      "v_max3_f32 v105, v105, v72, v73\n\t"
      "v_max3_f32 v104, v104, v74, v75\n\t"
      "v_max3_f32 v105, v105, v76, v77\n\t"
      "v_max3_f32 v104, v104, v78, v79\n\t"
      "v_max_f32 v104, v104, v105\n\t"
      "v_cmp_lt_f32 vcc, s41, v104\n\t"
      "v_mfma_f32_32x32x16_bf16 v[80:95], v[116:119], %[q1], v[144:159]\n\t"
      "s_cbranch_vccnz L_rare10_%=\n\t"
      "L_back10_%=:\n\t"
      "v_exp_f32 v64, v64\n\t"
      "v_exp_f32 v65, v65\n\t"
      "v_exp_f32 v66, v66\n\t"
      "v_exp_f32 v67, v67\n\t"
      "v_exp_f32 v68, v68\n\t"
      "v_exp_f32 v69, v69\n\t"
      "v_exp_f32 v70, v70\n\t"
      "v_exp_f32 v71, v71\n\t"
      "v_exp_f32 v72, v72\n\t"
      "v_cvt_pk_bf16_f32 v96, v64, v65\n\t"
      "v_cvt_pk_bf16_f32 v97, v66, v67\n\t"
      "v_cvt_pk_bf16_f32 v98, v68, v69\n\t"
      "v_cvt_pk_bf16_f32 v99, v70, v71\n\t"
      "v_exp_f32 v73, v73\n\t"
      "v_exp_f32 v74, v74\n\t"
      "v_mfma_f32_32x32x16_bf16 a[0:15], v[48:51], v[96:99], a[0:15]\n\t"
      "v_exp_f32 v75, v75\n\t"
      "v_exp_f32 v76, v76\n\t"
      "v_exp_f32 v77, v77\n\t"
      "v_exp_f32 v78, v78\n\t"
      "v_exp_f32 v79, v79\n\t"
      "v_cvt_pk_bf16_f32 v100, v72, v73\n\t"
      "v_cvt_pk_bf16_f32 v101, v74, v75\n\t"
      "v_cvt_pk_bf16_f32 v102, v76, v77\n\t"
      "v_cvt_pk_bf16_f32 v103, v78, v79\n\t"
      "v_max3_f32 v104, v80, v81, v82\n\t"
      "v_max3_f32 v105, v83, v84, v85\n\t"
      "v_mfma_f32_32x32x16_bf16 a[0:15], v[52:55], v[100:103], a[0:15]\n\t"
      "v_add_u32 %[vp], %[vp], %[vs]\n\t"
      "ds_read_b128 v[112:115], %[kp] offset:2048\n\t"
      "ds_read_b128 v[120:123], %[vp]\n\t"
      "ds_read_b128 v[124:127], %[vp] offset:512\n\t"
      "v_max3_f32 v104, v104, v86, v87\n\t"
      "v_max3_f32 v105, v105, v88, v89\n\t"
      "v_max3_f32 v104, v104, v90, v91\n\t"
      "v_max3_f32 v105, v105, v92, v93\n\t"
      "v_max3_f32 v104, v104, v94, v95\n\t"
      "v_max_f32 v104, v104, v105\n\t"
      "v_cmp_lt_f32 vcc, s41, v104\n\t"
      "v_mfma_f32_32x32x16_bf16 v[64:79], v[116:119], %[q2], v[160:175]\n\t"
      "s_cbranch_vccnz L_rare11_%=\n\t"
      "L_back11_%=:\n\t"
      "v_exp_f32 v80, v80\n\t"
      "v_exp_f32 v81, v81\n\t"
      "v_exp_f32 v82, v82\n\t"
      "v_exp_f32 v83, v83\n\t"
      "v_exp_f32 v84, v84\n\t"
      "v_exp_f32 v85, v85\n\t"
      "v_exp_f32 v86, v86\n\t"
      "v_exp_f32 v87, v87\n\t"
      "v_exp_f32 v88, v88\n\t"
      "v_cvt_pk_bf16_f32 v96, v80, v81\n\t"
      "v_cvt_pk_bf16_f32 v97, v82, v83\n\t"
      "v_cvt_pk_bf16_f32 v98, v84, v85\n\t"
      "v_cvt_pk_bf16_f32 v99, v86, v87\n\t"
      "v_exp_f32 v89, v89\n\t"
      "v_exp_f32 v90, v90\n\t"
      "v_mfma_f32_32x32x16_bf16 a[16:31], v[48:51], v[96:99], a[16:31]\n\t"
      "v_exp_f32 v91, v91\n\t"
      "v_exp_f32 v92, v92\n\t"
      "v_exp_f32 v93, v93\n\t"
      "v_exp_f32 v94, v94\n\t"
      "v_exp_f32 v95, v95\n\t"
      "v_cvt_pk_bf16_f32 v100, v88, v89\n\t"
      "v_cvt_pk_bf16_f32 v101, v90, v91\n\t"
      "v_cvt_pk_bf16_f32 v102, v92, v93\n\t"
      "v_cvt_pk_bf16_f32 v103, v94, v95\n\t"
      "v_max3_f32 v104, v64, v65, v66\n\t"
      "v_max3_f32 v105, v67, v68, v69\n\t"
      "v_mfma_f32_32x32x16_bf16 a[16:31], v[52:55], v[100:103], a[16:31]\n\t"
      "v_max3_f32 v104, v104, v70, v71\n\t"
      "v_max3_f32 v105, v105, v72, v73\n\t"
      "v_max3_f32 v104, v104, v74, v75\n\t"
      "v_max3_f32 v105, v105, v76, v77\n\t"
      "v_max3_f32 v104, v104, v78, v79\n\t"
      "v_max_f32 v104, v104, v105\n\t"
      "v_cmp_lt_f32 vcc, s41, v104\n\t"
      "v_mfma_f32_32x32x16_bf16 v[80:95], v[116:119], %[q3], v[176:191]\n\t"
      "s_cbranch_vccnz L_rare12_%=\n\t"
      "L_back12_%=:\n\t"
      "v_exp_f32 v64, v64\n\t"
      "v_exp_f32 v65, v65\n\t"
      "v_exp_f32 v66, v66\n\t"
      "v_exp_f32 v67, v67\n\t"
      "v_exp_f32 v68, v68\n\t"
      "v_exp_f32 v69, v69\n\t"
      "v_exp_f32 v70, v70\n\t"
      "v_exp_f32 v71, v71\n\t"
      "v_exp_f32 v72, v72\n\t"
      "v_cvt_pk_bf16_f32 v96, v64, v65\n\t"
      "v_cvt_pk_bf16_f32 v97, v66, v67\n\t"
      "v_cvt_pk_bf16_f32 v98, v68, v69\n\t"
      "v_cvt_pk_bf16_f32 v99, v70, v71\n\t"
      "v_exp_f32 v73, v73\n\t"
      "v_exp_f32 v74, v74\n\t"
      "v_mfma_f32_32x32x16_bf16 a[32:47], v[48:51], v[96:99], a[32:47]\n\t"
      "v_exp_f32 v75, v75\n\t"
      "v_exp_f32 v76, v76\n\t"
      "v_exp_f32 v77, v77\n\t"
      "v_exp_f32 v78, v78\n\t"
      "v_exp_f32 v79, v79\n\t"
      "v_cvt_pk_bf16_f32 v100, v72, v73\n\t"
      "v_cvt_pk_bf16_f32 v101, v74, v75\n\t"
      "v_cvt_pk_bf16_f32 v102, v76, v77\n\t"
      "v_cvt_pk_bf16_f32 v103, v78, v79\n\t"
      "v_max3_f32 v104, v80, v81, v82\n\t"
      "v_max3_f32 v105, v83, v84, v85\n\t"
      "v_mfma_f32_32x32x16_bf16 a[32:47], v[52:55], v[100:103], a[32:47]\n\t"
      "v_max3_f32 v104, v104, v86, v87\n\t"
      "v_max3_f32 v105, v105, v88, v89\n\t"
      "v_max3_f32 v104, v104, v90, v91\n\t"
      "v_max3_f32 v105, v105, v92, v93\n\t"
      "v_max3_f32 v104, v104, v94, v95\n\t"
      "v_max_f32 v104, v104, v105\n\t"
      "v_cmp_lt_f32 vcc, s41, v104\n\t"
      "v_mfma_f32_32x32x16_bf16 v[64:79], v[116:119], %[q4], v[192:207]\n\t"
      "s_cbranch_vccnz L_rare13_%=\n\t"
      "L_back13_%=:\n\t"
      "v_exp_f32 v80, v80\n\t"
      "v_exp_f32 v81, v81\n\t"
      "v_exp_f32 v82, v82\n\t"
      "v_exp_f32 v83, v83\n\t"
      "v_exp_f32 v84, v84\n\t"
      "v_exp_f32 v85, v85\n\t"
      "v_exp_f32 v86, v86\n\t"
      "v_exp_f32 v87, v87\n\t"
      "v_exp_f32 v88, v88\n\t"
      "v_cvt_pk_bf16_f32 v96, v80, v81\n\t"
      "v_cvt_pk_bf16_f32 v97, v82, v83\n\t"
      "v_cvt_pk_bf16_f32 v98, v84, v85\n\t"
      "v_cvt_pk_bf16_f32 v99, v86, v87\n\t"
      "v_exp_f32 v89, v89\n\t"
      "v_exp_f32 v90, v90\n\t"
      "v_mfma_f32_32x32x16_bf16 a[48:63], v[48:51], v[96:99], a[48:63]\n\t"
      "v_exp_f32 v91, v91\n\t"
      "v_exp_f32 v92, v92\n\t"
      "v_exp_f32 v93, v93\n\t"
      "v_exp_f32 v94, v94\n\t"
      "v_exp_f32 v95, v95\n\t"
      "v_cvt_pk_bf16_f32 v100, v88, v89\n\t"
      "v_cvt_pk_bf16_f32 v101, v90, v91\n\t"
      "v_cvt_pk_bf16_f32 v102, v92, v93\n\t"
      "v_cvt_pk_bf16_f32 v103, v94, v95\n\t"
      "v_max3_f32 v104, v64, v65, v66\n\t"
      "v_max3_f32 v105, v67, v68, v69\n\t"
      "v_mfma_f32_32x32x16_bf16 a[48:63], v[52:55], v[100:103], a[48:63]\n\t"
      "v_max3_f32 v104, v104, v70, v71\n\t"
      "v_max3_f32 v105, v105, v72, v73\n\t"
      "v_max3_f32 v104, v104, v74, v75\n\t"
      "v_max3_f32 v105, v105, v76, v77\n\t"
      "v_max3_f32 v104, v104, v78, v79\n\t"
      "v_max_f32 v104, v104, v105\n\t"
      "v_cmp_lt_f32 vcc, s41, v104\n\t"
      "v_mfma_f32_32x32x16_bf16 v[80:95], v[116:119], %[q5], v[208:223]\n\t"
      "s_cbranch_vccnz L_rare14_%=\n\t"
      "L_back14_%=:\n\t"
      "v_exp_f32 v64, v64\n\t"
      "v_exp_f32 v65, v65\n\t"
      "v_exp_f32 v66, v66\n\t"
      "v_exp_f32 v67, v67\n\t"
      "v_exp_f32 v68, v68\n\t"
      "v_exp_f32 v69, v69\n\t"
      "v_exp_f32 v70, v70\n\t"
      "v_exp_f32 v71, v71\n\t"
      "v_exp_f32 v72, v72\n\t"
      "v_cvt_pk_bf16_f32 v96, v64, v65\n\t"
      "v_cvt_pk_bf16_f32 v97, v66, v67\n\t"
      "v_cvt_pk_bf16_f32 v98, v68, v69\n\t"
      "v_cvt_pk_bf16_f32 v99, v70, v71\n\t"
      "v_exp_f32 v73, v73\n\t"
      "v_exp_f32 v74, v74\n\t"
      "v_mfma_f32_32x32x16_bf16 a[64:79], v[48:51], v[96:99], a[64:79]\n\t"
      "v_exp_f32 v75, v75\n\t"
      "v_exp_f32 v76, v76\n\t"
      "v_exp_f32 v77, v77\n\t"
      "v_exp_f32 v78, v78\n\t"
      "v_exp_f32 v79, v79\n\t"
      "v_cvt_pk_bf16_f32 v100, v72, v73\n\t"
      "v_cvt_pk_bf16_f32 v101, v74, v75\n\t"
      "v_cvt_pk_bf16_f32 v102, v76, v77\n\t"
      "v_cvt_pk_bf16_f32 v103, v78, v79\n\t"
      "v_max3_f32 v104, v80, v81, v82\n\t"
      "v_max3_f32 v105, v83, v84, v85\n\t"
      "v_mfma_f32_32x32x16_bf16 a[64:79], v[52:55], v[100:103], a[64:79]\n\t"
      "v_max3_f32 v104, v104, v86, v87\n\t"
      "v_max3_f32 v105, v105, v88, v89\n\t"
      "v_max3_f32 v104, v104, v90, v91\n\t"
      "v_max3_f32 v105, v105, v92, v93\n\t"
      "v_max3_f32 v104, v104, v94, v95\n\t"
      "v_max_f32 v104, v104, v105\n\t"
      "v_cmp_lt_f32 vcc, s41, v104\n\t"
      "v_mfma_f32_32x32x16_bf16 v[64:79], v[116:119], %[q6], v[224:239]\n\t"
      "s_cbranch_vccnz L_rare15_%=\n\t"
      "L_back15_%=:\n\t"
      "v_exp_f32 v80, v80\n\t"
      "v_exp_f32 v81, v81\n\t"
      "v_exp_f32 v82, v82\n\t"
      "v_exp_f32 v83, v83\n\t"
      "v_exp_f32 v84, v84\n\t"
      "v_exp_f32 v85, v85\n\t"
      "v_exp_f32 v86, v86\n\t"
      "v_exp_f32 v87, v87\n\t"
      "v_exp_f32 v88, v88\n\t"
      "v_cvt_pk_bf16_f32 v96, v80, v81\n\t"
      "v_cvt_pk_bf16_f32 v97, v82, v83\n\t"
      "v_cvt_pk_bf16_f32 v98, v84, v85\n\t"
      "v_cvt_pk_bf16_f32 v99, v86, v87\n\t"
      "v_exp_f32 v89, v89\n\t"
      "v_exp_f32 v90, v90\n\t"
      "v_mfma_f32_32x32x16_bf16 a[80:95], v[48:51], v[96:99], a[80:95]\n\t"
      "v_exp_f32 v91, v91\n\t"
      "v_exp_f32 v92, v92\n\t"
      "v_exp_f32 v93, v93\n\t"
      "v_exp_f32 v94, v94\n\t"
      "v_exp_f32 v95, v95\n\t"
      "v_cvt_pk_bf16_f32 v100, v88, v89\n\t"
      "v_cvt_pk_bf16_f32 v101, v90, v91\n\t"
      "v_cvt_pk_bf16_f32 v102, v92, v93\n\t"
      "v_cvt_pk_bf16_f32 v103, v94, v95\n\t"
      "v_max3_f32 v104, v64, v65, v66\n\t"
      "v_max3_f32 v105, v67, v68, v69\n\t"
      "v_mfma_f32_32x32x16_bf16 a[80:95], v[52:55], v[100:103], a[80:95]\n\t"
      "v_max3_f32 v104, v104, v70, v71\n\t"
      "v_max3_f32 v105, v105, v72, v73\n\t"
      "v_max3_f32 v104, v104, v74, v75\n\t"
      "v_max3_f32 v105, v105, v76, v77\n\t"
      "v_max3_f32 v104, v104, v78, v79\n\t"
      "v_max_f32 v104, v104, v105\n\t"
      "v_cmp_lt_f32 vcc, s41, v104\n\t"
      "v_mfma_f32_32x32x16_bf16 v[80:95], v[116:119], %[q7], v[240:255]\n\t"
      "s_cbranch_vccnz L_rare16_%=\n\t"
      "L_back16_%=:\n\t"
      "v_exp_f32 v64, v64\n\t"
      "v_exp_f32 v65, v65\n\t"
      "v_exp_f32 v66, v66\n\t"
      "v_exp_f32 v67, v67\n\t"
      "v_exp_f32 v68, v68\n\t"
      "v_exp_f32 v69, v69\n\t"
      "v_exp_f32 v70, v70\n\t"
      "v_exp_f32 v71, v71\n\t"
      "v_exp_f32 v72, v72\n\t"
      "v_cvt_pk_bf16_f32 v96, v64, v65\n\t"
      "v_cvt_pk_bf16_f32 v97, v66, v67\n\t"
      "v_cvt_pk_bf16_f32 v98, v68, v69\n\t"
      "v_cvt_pk_bf16_f32 v99, v70, v71\n\t"
      "v_exp_f32 v73, v73\n\t"
      "v_exp_f32 v74, v74\n\t"
      "v_mfma_f32_32x32x16_bf16 a[96:111], v[48:51], v[96:99], a[96:111]\n\t"
      "v_exp_f32 v75, v75\n\t"
      "v_exp_f32 v76, v76\n\t"
      "v_exp_f32 v77, v77\n\t"
      "v_exp_f32 v78, v78\n\t"
      "v_exp_f32 v79, v79\n\t"
      "v_cvt_pk_bf16_f32 v100, v72, v73\n\t"
      "v_cvt_pk_bf16_f32 v101, v74, v75\n\t"
      "v_cvt_pk_bf16_f32 v102, v76, v77\n\t"
      "v_cvt_pk_bf16_f32 v103, v78, v79\n\t"
      "v_max3_f32 v104, v80, v81, v82\n\t"
      "v_max3_f32 v105, v83, v84, v85\n\t"
      "v_mfma_f32_32x32x16_bf16 a[96:111], v[52:55], v[100:103], a[96:111]\n\t"
      "v_max3_f32 v104, v104, v86, v87\n\t"
      "v_max3_f32 v105, v105, v88, v89\n\t"
      "v_max3_f32 v104, v104, v90, v91\n\t"
      "v_max3_f32 v105, v105, v92, v93\n\t"
      "v_max3_f32 v104, v104, v94, v95\n\t"
      "v_max_f32 v104, v104, v105\n\t"
      "v_cmp_lt_f32 vcc, s41, v104\n\t"
      "s_waitcnt lgkmcnt(2)\n\t"
      "v_mfma_f32_32x32x16_bf16 v[64:79], v[112:115], %[q0], v[128:143]\n\t"
      "s_cbranch_vccnz L_rare17_%=\n\t"
      "L_back17_%=:\n\t"
      "v_exp_f32 v80, v80\n\t"
      "v_exp_f32 v81, v81\n\t"
      "v_exp_f32 v82, v82\n\t"
      "v_exp_f32 v83, v83\n\t"
      "v_exp_f32 v84, v84\n\t"
      "v_exp_f32 v85, v85\n\t"
      "v_exp_f32 v86, v86\n\t"
      "v_exp_f32 v87, v87\n\t"
      "v_exp_f32 v88, v88\n\t"
      "v_cvt_pk_bf16_f32 v96, v80, v81\n\t"
      "v_cvt_pk_bf16_f32 v97, v82, v83\n\t"
      "v_cvt_pk_bf16_f32 v98, v84, v85\n\t"
      "v_cvt_pk_bf16_f32 v99, v86, v87\n\t"
      "v_exp_f32 v89, v89\n\t"
      "v_exp_f32 v90, v90\n\t"
      "v_mfma_f32_32x32x16_bf16 a[112:127], v[48:51], v[96:99], a[112:127]\n\t"
      "v_exp_f32 v91, v91\n\t"
      "v_exp_f32 v92, v92\n\t"
      "v_exp_f32 v93, v93\n\t"
      "v_exp_f32 v94, v94\n\t"
      "v_exp_f32 v95, v95\n\t"
      "v_cvt_pk_bf16_f32 v100, v88, v89\n\t"
      "v_cvt_pk_bf16_f32 v101, v90, v91\n\t"
      "v_cvt_pk_bf16_f32 v102, v92, v93\n\t"
      "v_cvt_pk_bf16_f32 v103, v94, v95\n\t"
      "s_waitcnt lgkmcnt(0)\n\t"
      "v_add_u32 %[kp], 0x800, %[kp]\n\t"
      "s_sub_u32 s40, s40, 1\n\t"
      "s_cmp_lg_u32 s40, 0\n\t"
      "s_cbranch_scc1 L_tops_%=\n\t"
      "v_mfma_f32_32x32x16_bf16 a[112:127], v[52:55], v[100:103], a[112:127]\n\t"
      "s_branch L_fin_%=\n\t"
      "L_rare00_%=:\n\t"
      "s_nop 15\n\t"
      "v_mov_b32 v105, v104\n\t"
      "s_nop 1\n\t"
      "v_permlane32_swap_b32 v104, v105\n\t"
      "v_max3_f32 v104, v104, v105, 0\n\t"
      "v_exp_f32 v105, -v104\n\t"
      "v_sub_f32 v64, v64, v104\n\t"
      "v_sub_f32 v128, v128, v104\n\t"
      "v_sub_f32 v65, v65, v104\n\t"
      "v_sub_f32 v129, v129, v104\n\t"
      "v_sub_f32 v66, v66, v104\n\t"
      "v_sub_f32 v130, v130, v104\n\t"
      "v_sub_f32 v67, v67, v104\n\t"
      "v_sub_f32 v131, v131, v104\n\t"
      "v_sub_f32 v68, v68, v104\n\t"
      "v_sub_f32 v132, v132, v104\n\t"
      "v_sub_f32 v69, v69, v104\n\t"
      "v_sub_f32 v133, v133, v104\n\t"
      "v_sub_f32 v70, v70, v104\n\t"
      "v_sub_f32 v134, v134, v104\n\t"
      "v_sub_f32 v71, v71, v104\n\t"
      "v_sub_f32 v135, v135, v104\n\t"
      "v_sub_f32 v72, v72, v104\n\t"
      "v_sub_f32 v136, v136, v104\n\t"
      "v_sub_f32 v73, v73, v104\n\t"
      "v_sub_f32 v137, v137, v104\n\t"
      "v_sub_f32 v74, v74, v104\n\t"
      "v_sub_f32 v138, v138, v104\n\t"
      "v_sub_f32 v75, v75, v104\n\t"
      "v_sub_f32 v139, v139, v104\n\t"
      "v_sub_f32 v76, v76, v104\n\t"
      "v_sub_f32 v140, v140, v104\n\t"
      "v_sub_f32 v77, v77, v104\n\t"
      "v_sub_f32 v141, v141, v104\n\t"
      "v_sub_f32 v78, v78, v104\n\t"
      "v_sub_f32 v142, v142, v104\n\t"
      "v_sub_f32 v79, v79, v104\n\t"
      "v_sub_f32 v143, v143, v104\n\t"
      "v_accvgpr_read_b32 v106, a0\n\t"
      "v_mul_f32 v106, v106, v105\n\t"
      "v_accvgpr_write_b32 a0, v106\n\t"
      "v_accvgpr_read_b32 v106, a1\n\t"
      "v_mul_f32 v106, v106, v105\n\t"
      "v_accvgpr_write_b32 a1, v106\n\t"
      "v_accvgpr_read_b32 v106, a2\n\t"
      "v_mul_f32 v106, v106, v105\n\t"
      "v_accvgpr_write_b32 a2, v106\n\t"
      "v_accvgpr_read_b32 v106, a3\n\t"
      "v_mul_f32 v106, v106, v105\n\t"
      "v_accvgpr_write_b32 a3, v106\n\t"
      "v_accvgpr_read_b32 v106, a4\n\t"
      "v_mul_f32 v106, v106, v105\n\t"
      "v_accvgpr_write_b32 a4, v106\n\t"
      "v_accvgpr_read_b32 v106, a5\n\t"
      "v_mul_f32 v106, v106, v105\n\t"
      "v_accvgpr_write_b32 a5, v106\n\t"
      "v_accvgpr_read_b32 v106, a6\n\t"
      "v_mul_f32 v106, v106, v105\n\t"
      "v_accvgpr_write_b32 a6, v106\n\t"
      "v_accvgpr_read_b32 v106, a7\n\t"
      "v_mul_f32 v106, v106, v105\n\t"
      "v_accvgpr_write_b32 a7, v106\n\t"
      "v_accvgpr_read_b32 v106, a8\n\t"
      "v_mul_f32 v106, v106, v105\n\t"
      "v_accvgpr_write_b32 a8, v106\n\t"
      "s_branch L_back00_%=\n\t"
      "L_rare01_%=:\n\t"
      "s_nop 15\n\t"
      "v_mov_b32 v105, v104\n\t"
      "s_nop 1\n\t"
      "v_permlane32_swap_b32 v104, v105\n\t"
      "v_max3_f32 v104, v104, v105, 0\n\t"
      "v_exp_f32 v105, -v104\n\t"
      "v_sub_f32 v80, v80, v104\n\t"
      "v_sub_f32 v144, v144, v104\n\t"
      "v_sub_f32 v81, v81, v104\n\t"
      "v_sub_f32 v145, v145, v104\n\t"
      "v_sub_f32 v82, v82, v104\n\t"
      "v_sub_f32 v146, v146, v104\n\t"
      "v_sub_f32 v83, v83, v104\n\t"
      "v_sub_f32 v147, v147, v104\n\t"
      "v_sub_f32 v84, v84, v104\n\t"
      "v_sub_f32 v148, v148, v104\n\t"
      "v_sub_f32 v85, v85, v104\n\t"
      "v_sub_f32 v149, v149, v104\n\t"
      "v_sub_f32 v86, v86, v104\n\t"
      "v_sub_f32 v150, v150, v104\n\t"
      "v_sub_f32 v87, v87, v104\n\t"
      "v_sub_f32 v151, v151, v104\n\t"
      "v_sub_f32 v88, v88, v104\n\t"
      "v_sub_f32 v152, v152, v104\n\t"
      "v_sub_f32 v89, v89, v104\n\t"
      "v_sub_f32 v153, v153, v104\n\t"
      "v_sub_f32 v90, v90, v104\n\t"
      "v_sub_f32 v154, v154, v104\n\t"
      "v_sub_f32 v91, v91, v104\n\t"
      "v_sub_f32 v155, v155, v104\n\t"
      "v_sub_f32 v92, v92, v104\n\t"
      "v_sub_f32 v156, v156, v104\n\t"
      "v_sub_f32 v93, v93, v104\n\t"
      "v_sub_f32 v157, v157, v104\n\t"
      "v_sub_f32 v94, v94, v104\n\t"
      "v_sub_f32 v158, v158, v104\n\t"
      "v_sub_f32 v95, v95, v104\n\t"
      "v_sub_f32 v159, v159, v104\n\t"
      "v_accvgpr_read_b32 v106, a16\n\t"
      "v_mul_f32 v106, v106, v105\n\t"
      "v_accvgpr_write_b32 a16, v106\n\t"
      "v_accvgpr_read_b32 v106, a17\n\t"
      "v_mul_f32 v106, v106, v105\n\t"
      "v_accvgpr_write_b32 a17, v106\n\t"
      "v_accvgpr_read_b32 v106, a18\n\t"
      "v_mul_f32 v106, v106, v105\n\t"
      "v_accvgpr_write_b32 a18, v106\n\t"
      "v_accvgpr_read_b32 v106, a19\n\t"
      "v_mul_f32 v106, v106, v105\n\t"
      "v_accvgpr_write_b32 a19, v106\n\t"
      "v_accvgpr_read_b32 v106, a20\n\t"
      "v_mul_f32 v106, v106, v105\n\t"
      "v_accvgpr_write_b32 a20, v106\n\t"
      "v_accvgpr_read_b32 v106, a21\n\t"
      "v_mul_f32 v106, v106, v105\n\t"
      "v_accvgpr_write_b32 a21, v106\n\t"
      "v_accvgpr_read_b32 v106, a22\n\t"
      "v_mul_f32 v106, v106, v105\n\t"
      "v_accvgpr_write_b32 a22, v106\n\t"
      "v_accvgpr_read_b32 v106, a23\n\t"
      "v_mul_f32 v106, v106, v105\n\t"
      "v_accvgpr_write_b32 a23, v106\n\t"
      "v_accvgpr_read_b32 v106, a24\n\t"
      "v_mul_f32 v106, v106, v105\n\t"
      "v_accvgpr_write_b32 a24, v106\n\t"
      "s_branch L_back01_%=\n\t"
      "L_rare02_%=:\n\t"
      "s_nop 15\n\t"
      "v_mov_b32 v105, v104\n\t"
      "s_nop 1\n\t"
      "v_permlane32_swap_b32 v104, v105\n\t"
      "v_max3_f32 v104, v104, v105, 0\n\t"
      "v_exp_f32 v105, -v104\n\t"
      "v_sub_f32 v64, v64, v104\n\t"
      "v_sub_f32 v160, v160, v104\n\t"
      "v_sub_f32 v65, v65, v104\n\t"
      "v_sub_f32 v161, v161, v104\n\t"
      "v_sub_f32 v66, v66, v104\n\t"
      "v_sub_f32 v162, v162, v104\n\t"
      "v_sub_f32 v67, v67, v104\n\t"
      "v_sub_f32 v163, v163, v104\n\t"
      "v_sub_f32 v68, v68, v104\n\t"
      "v_sub_f32 v164, v164, v104\n\t"
      "v_sub_f32 v69, v69, v104\n\t"
      "v_sub_f32 v165, v165, v104\n\t"
      "v_sub_f32 v70, v70, v104\n\t"
      "v_sub_f32 v166, v166, v104\n\t"
      "v_sub_f32 v71, v71, v104\n\t"
      "v_sub_f32 v167, v167, v104\n\t"
      "v_sub_f32 v72, v72, v104\n\t"
      "v_sub_f32 v168, v168, v104\n\t"
      "v_sub_f32 v73, v73, v104\n\t"
      "v_sub_f32 v169, v169, v104\n\t"
      "v_sub_f32 v74, v74, v104\n\t"
      "v_sub_f32 v170, v170, v104\n\t"
      "v_sub_f32 v75, v75, v104\n\t"
      "v_sub_f32 v171, v171, v104\n\t"
      "v_sub_f32 v76, v76, v104\n\t"
      "v_sub_f32 v172, v172, v104\n\t"
      "v_sub_f32 v77, v77, v104\n\t"
      "v_sub_f32 v173, v173, v104\n\t"
      "v_sub_f32 v78, v78, v104\n\t"
      "v_sub_f32 v174, v174, v104\n\t"
      "v_sub_f32 v79, v79, v104\n\t"
      "v_sub_f32 v175, v175, v104\n\t"
      "v_accvgpr_read_b32 v106, a32\n\t"
      "v_mul_f32 v106, v106, v105\n\t"
      "v_accvgpr_write_b32 a32, v106\n\t"
      "v_accvgpr_read_b32 v106, a33\n\t"
      "v_mul_f32 v106, v106, v105\n\t"
      "v_accvgpr_write_b32 a33, v106\n\t"
      "v_accvgpr_read_b32 v106, a34\n\t"
      "v_mul_f32 v106, v106, v105\n\t"
      "v_accvgpr_write_b32 a34, v106\n\t"
      "v_accvgpr_read_b32 v106, a35\n\t"
      "v_mul_f32 v106, v106, v105\n\t"
      "v_accvgpr_write_b32 a35, v106\n\t"
      "v_accvgpr_read_b32 v106, a36\n\t"
      "v_mul_f32 v106, v106, v105\n\t"
      "v_accvgpr_write_b32 a36, v106\n\t"
      "v_accvgpr_read_b32 v106, a37\n\t"
      "v_mul_f32 v106, v106, v105\n\t"
      "v_accvgpr_write_b32 a37, v106\n\t"
      "v_accvgpr_read_b32 v106, a38\n\t"
      "v_mul_f32 v106, v106, v105\n\t"
      "v_accvgpr_write_b32 a38, v106\n\t"
      "v_accvgpr_read_b32 v106, a39\n\t"
      "v_mul_f32 v106, v106, v105\n\t"
      "v_accvgpr_write_b32 a39, v106\n\t"
      "v_accvgpr_read_b32 v106, a40\n\t"
      "v_mul_f32 v106, v106, v105\n\t"
      "v_accvgpr_write_b32 a40, v106\n\t"
      "s_branch L_back02_%=\n\t"
      "L_rare03_%=:\n\t"
      "s_nop 15\n\t"
      "v_mov_b32 v105, v104\n\t"
      "s_nop 1\n\t"
      "v_permlane32_swap_b32 v104, v105\n\t"
      "v_max3_f32 v104, v104, v105, 0\n\t"
      "v_exp_f32 v105, -v104\n\t"
      "v_sub_f32 v80, v80, v104\n\t"
      "v_sub_f32 v176, v176, v104\n\t"
      "v_sub_f32 v81, v81, v104\n\t"
      "v_sub_f32 v177, v177, v104\n\t"
      "v_sub_f32 v82, v82, v104\n\t"
      "v_sub_f32 v178, v178, v104\n\t"
      "v_sub_f32 v83, v83, v104\n\t"
      "v_sub_f32 v179, v179, v104\n\t"
      "v_sub_f32 v84, v84, v104\n\t"
      "v_sub_f32 v180, v180, v104\n\t"
      "v_sub_f32 v85, v85, v104\n\t"
      "v_sub_f32 v181, v181, v104\n\t"
      "v_sub_f32 v86, v86, v104\n\t"
      "v_sub_f32 v182, v182, v104\n\t"
      "v_sub_f32 v87, v87, v104\n\t"
      "v_sub_f32 v183, v183, v104\n\t"
      "v_sub_f32 v88, v88, v104\n\t"
      "v_sub_f32 v184, v184, v104\n\t"
      "v_sub_f32 v89, v89, v104\n\t"
      "v_sub_f32 v185, v185, v104\n\t"
      "v_sub_f32 v90, v90, v104\n\t"
      "v_sub_f32 v186, v186, v104\n\t"
      "v_sub_f32 v91, v91, v104\n\t"
      "v_sub_f32 v187, v187, v104\n\t"
      "v_sub_f32 v92, v92, v104\n\t"
      "v_sub_f32 v188, v188, v104\n\t"
      "v_sub_f32 v93, v93, v104\n\t"
      "v_sub_f32 v189, v189, v104\n\t"
      "v_sub_f32 v94, v94, v104\n\t"
      "v_sub_f32 v190, v190, v104\n\t"
      "v_sub_f32 v95, v95, v104\n\t"
      "v_sub_f32 v191, v191, v104\n\t"
      "v_accvgpr_read_b32 v106, a48\n\t"
      "v_mul_f32 v106, v106, v105\n\t"
      "v_accvgpr_write_b32 a48, v106\n\t"
      "v_accvgpr_read_b32 v106, a49\n\t"
      "v_mul_f32 v106, v106, v105\n\t"
      "v_accvgpr_write_b32 a49, v106\n\t"
      "v_accvgpr_read_b32 v106, a50\n\t"
      "v_mul_f32 v106, v106, v105\n\t"
      "v_accvgpr_write_b32 a50, v106\n\t"
      "v_accvgpr_read_b32 v106, a51\n\t"
      "v_mul_f32 v106, v106, v105\n\t"
      "v_accvgpr_write_b32 a51, v106\n\t"
      "v_accvgpr_read_b32 v106, a52\n\t"
      "v_mul_f32 v106, v106, v105\n\t"
      "v_accvgpr_write_b32 a52, v106\n\t"
      "v_accvgpr_read_b32 v106, a53\n\t"
      "v_mul_f32 v106, v106, v105\n\t"
      "v_accvgpr_write_b32 a53, v106\n\t"
      "v_accvgpr_read_b32 v106, a54\n\t"
      "v_mul_f32 v106, v106, v105\n\t"
      "v_accvgpr_write_b32 a54, v106\n\t"
      "v_accvgpr_read_b32 v106, a55\n\t"
      "v_mul_f32 v106, v106, v105\n\t"
      "v_accvgpr_write_b32 a55, v106\n\t"
      "v_accvgpr_read_b32 v106, a56\n\t"
      "v_mul_f32 v106, v106, v105\n\t"
      "v_accvgpr_write_b32 a56, v106\n\t"
      "s_branch L_back03_%=\n\t"
      "L_rare04_%=:\n\t"
      "s_nop 15\n\t"
      "v_mov_b32 v105, v104\n\t"
      "s_nop 1\n\t"
      "v_permlane32_swap_b32 v104, v105\n\t"
      "v_max3_f32 v104, v104, v105, 0\n\t"
      "v_exp_f32 v105, -v104\n\t"
      "v_sub_f32 v64, v64, v104\n\t"
      "v_sub_f32 v192, v192, v104\n\t"
      "v_sub_f32 v65, v65, v104\n\t"
      "v_sub_f32 v193, v193, v104\n\t"
      "v_sub_f32 v66, v66, v104\n\t"
      "v_sub_f32 v194, v194, v104\n\t"
      "v_sub_f32 v67, v67, v104\n\t"
      "v_sub_f32 v195, v195, v104\n\t"
      "v_sub_f32 v68, v68, v104\n\t"
      "v_sub_f32 v196, v196, v104\n\t"
      "v_sub_f32 v69, v69, v104\n\t"
      "v_sub_f32 v197, v197, v104\n\t"
      "v_sub_f32 v70, v70, v104\n\t"
      "v_sub_f32 v198, v198, v104\n\t"
      "v_sub_f32 v71, v71, v104\n\t"
      "v_sub_f32 v199, v199, v104\n\t"
      "v_sub_f32 v72, v72, v104\n\t"
      "v_sub_f32 v200, v200, v104\n\t"
      "v_sub_f32 v73, v73, v104\n\t"
      "v_sub_f32 v201, v201, v104\n\t"
      "v_sub_f32 v74, v74, v104\n\t"
      "v_sub_f32 v202, v202, v104\n\t"
      "v_sub_f32 v75, v75, v104\n\t"
      "v_sub_f32 v203, v203, v104\n\t"
      "v_sub_f32 v76, v76, v104\n\t"
      "v_sub_f32 v204, v204, v104\n\t"
      "v_sub_f32 v77, v77, v104\n\t"
      "v_sub_f32 v205, v205, v104\n\t"
      "v_sub_f32 v78, v78, v104\n\t"
      "v_sub_f32 v206, v206, v104\n\t"
      "v_sub_f32 v79, v79, v104\n\t"
      "v_sub_f32 v207, v207, v104\n\t"
      "v_accvgpr_read_b32 v106, a64\n\t"
      "v_mul_f32 v106, v106, v105\n\t"
      "v_accvgpr_write_b32 a64, v106\n\t"
      "v_accvgpr_read_b32 v106, a65\n\t"
      "v_mul_f32 v106, v106, v105\n\t"
      "v_accvgpr_write_b32 a65, v106\n\t"
      "v_accvgpr_read_b32 v106, a66\n\t"
      "v_mul_f32 v106, v106, v105\n\t"
      "v_accvgpr_write_b32 a66, v106\n\t"
      "v_accvgpr_read_b32 v106, a67\n\t"
      "v_mul_f32 v106, v106, v105\n\t"
      "v_accvgpr_write_b32 a67, v106\n\t"
      "v_accvgpr_read_b32 v106, a68\n\t"
      "v_mul_f32 v106, v106, v105\n\t"
      "v_accvgpr_write_b32 a68, v106\n\t"
      "v_accvgpr_read_b32 v106, a69\n\t"
      "v_mul_f32 v106, v106, v105\n\t"
      "v_accvgpr_write_b32 a69, v106\n\t"
      "v_accvgpr_read_b32 v106, a70\n\t"
      "v_mul_f32 v106, v106, v105\n\t"
      "v_accvgpr_write_b32 a70, v106\n\t"
      "v_accvgpr_read_b32 v106, a71\n\t"
      "v_mul_f32 v106, v106, v105\n\t"
      "v_accvgpr_write_b32 a71, v106\n\t"
      "v_accvgpr_read_b32 v106, a72\n\t"
      "v_mul_f32 v106, v106, v105\n\t"
      "v_accvgpr_write_b32 a72, v106\n\t"
      "s_branch L_back04_%=\n\t"
      "L_rare05_%=:\n\t"
      "s_nop 15\n\t"
      "v_mov_b32 v105, v104\n\t"
      "s_nop 1\n\t"
      "v_permlane32_swap_b32 v104, v105\n\t"
      "v_max3_f32 v104, v104, v105, 0\n\t"
      "v_exp_f32 v105, -v104\n\t"
      "v_sub_f32 v80, v80, v104\n\t"
      "v_sub_f32 v208, v208, v104\n\t"
      "v_sub_f32 v81, v81, v104\n\t"
      "v_sub_f32 v209, v209, v104\n\t"
      "v_sub_f32 v82, v82, v104\n\t"
      "v_sub_f32 v210, v210, v104\n\t"
      "v_sub_f32 v83, v83, v104\n\t"
      "v_sub_f32 v211, v211, v104\n\t"
      "v_sub_f32 v84, v84, v104\n\t"
      "v_sub_f32 v212, v212, v104\n\t"
      "v_sub_f32 v85, v85, v104\n\t"
      "v_sub_f32 v213, v213, v104\n\t"
      "v_sub_f32 v86, v86, v104\n\t"
      "v_sub_f32 v214, v214, v104\n\t"
      "v_sub_f32 v87, v87, v104\n\t"
      "v_sub_f32 v215, v215, v104\n\t"
      "v_sub_f32 v88, v88, v104\n\t"
      "v_sub_f32 v216, v216, v104\n\t"
      "v_sub_f32 v89, v89, v104\n\t"
      "v_sub_f32 v217, v217, v104\n\t"
      "v_sub_f32 v90, v90, v104\n\t"
      "v_sub_f32 v218, v218, v104\n\t"
      "v_sub_f32 v91, v91, v104\n\t"
      "v_sub_f32 v219, v219, v104\n\t"
      "v_sub_f32 v92, v92, v104\n\t"
      "v_sub_f32 v220, v220, v104\n\t"
      "v_sub_f32 v93, v93, v104\n\t"
      "v_sub_f32 v221, v221, v104\n\t"
      "v_sub_f32 v94, v94, v104\n\t"
      "v_sub_f32 v222, v222, v104\n\t"
      "v_sub_f32 v95, v95, v104\n\t"
      "v_sub_f32 v223, v223, v104\n\t"
      "v_accvgpr_read_b32 v106, a80\n\t"
      "v_mul_f32 v106, v106, v105\n\t"
      "v_accvgpr_write_b32 a80, v106\n\t"
      "v_accvgpr_read_b32 v106, a81\n\t"
      "v_mul_f32 v106, v106, v105\n\t"
      "v_accvgpr_write_b32 a81, v106\n\t"
      "v_accvgpr_read_b32 v106, a82\n\t"
      "v_mul_f32 v106, v106, v105\n\t"
      "v_accvgpr_write_b32 a82, v106\n\t"
      "v_accvgpr_read_b32 v106, a83\n\t"
      "v_mul_f32 v106, v106, v105\n\t"
      "v_accvgpr_write_b32 a83, v106\n\t"
      "v_accvgpr_read_b32 v106, a84\n\t"
      "v_mul_f32 v106, v106, v105\n\t"
      "v_accvgpr_write_b32 a84, v106\n\t"
      "v_accvgpr_read_b32 v106, a85\n\t"
      "v_mul_f32 v106, v106, v105\n\t"
      "v_accvgpr_write_b32 a85, v106\n\t"
      "v_accvgpr_read_b32 v106, a86\n\t"
      "v_mul_f32 v106, v106, v105\n\t"
      "v_accvgpr_write_b32 a86, v106\n\t"
      "v_accvgpr_read_b32 v106, a87\n\t"
      "v_mul_f32 v106, v106, v105\n\t"
      "v_accvgpr_write_b32 a87, v106\n\t"
      "v_accvgpr_read_b32 v106, a88\n\t"
      "v_mul_f32 v106, v106, v105\n\t"
      "v_accvgpr_write_b32 a88, v106\n\t"
      "s_branch L_back05_%=\n\t"
      "L_rare06_%=:\n\t"
      "s_nop 15\n\t"
      "v_mov_b32 v105, v104\n\t"
      "s_nop 1\n\t"
      "v_permlane32_swap_b32 v104, v105\n\t"
      "v_max3_f32 v104, v104, v105, 0\n\t"
      "v_exp_f32 v105, -v104\n\t"
      "v_sub_f32 v64, v64, v104\n\t"
      "v_sub_f32 v224, v224, v104\n\t"
      "v_sub_f32 v65, v65, v104\n\t"
      "v_sub_f32 v225, v225, v104\n\t"
      "v_sub_f32 v66, v66, v104\n\t"
      "v_sub_f32 v226, v226, v104\n\t"
      "v_sub_f32 v67, v67, v104\n\t"
      "v_sub_f32 v227, v227, v104\n\t"
      "v_sub_f32 v68, v68, v104\n\t"
      "v_sub_f32 v228, v228, v104\n\t"
      "v_sub_f32 v69, v69, v104\n\t"
      "v_sub_f32 v229, v229, v104\n\t"
      "v_sub_f32 v70, v70, v104\n\t"
      "v_sub_f32 v230, v230, v104\n\t"
      "v_sub_f32 v71, v71, v104\n\t"
      "v_sub_f32 v231, v231, v104\n\t"
      "v_sub_f32 v72, v72, v104\n\t"
      "v_sub_f32 v232, v232, v104\n\t"
      "v_sub_f32 v73, v73, v104\n\t"
      "v_sub_f32 v233, v233, v104\n\t"
      "v_sub_f32 v74, v74, v104\n\t"
      "v_sub_f32 v234, v234, v104\n\t"
      "v_sub_f32 v75, v75, v104\n\t"
      "v_sub_f32 v235, v235, v104\n\t"
      "v_sub_f32 v76, v76, v104\n\t"
      "v_sub_f32 v236, v236, v104\n\t"
      "v_sub_f32 v77, v77, v104\n\t"
      "v_sub_f32 v237, v237, v104\n\t"
      "v_sub_f32 v78, v78, v104\n\t"
      "v_sub_f32 v238, v238, v104\n\t"
      "v_sub_f32 v79, v79, v104\n\t"
      "v_sub_f32 v239, v239, v104\n\t"
      "v_accvgpr_read_b32 v106, a96\n\t"
      "v_mul_f32 v106, v106, v105\n\t"
      "v_accvgpr_write_b32 a96, v106\n\t"
      "v_accvgpr_read_b32 v106, a97\n\t"
      "v_mul_f32 v106, v106, v105\n\t"
      "v_accvgpr_write_b32 a97, v106\n\t"
      "v_accvgpr_read_b32 v106, a98\n\t"
      "v_mul_f32 v106, v106, v105\n\t"
      "v_accvgpr_write_b32 a98, v106\n\t"
      "v_accvgpr_read_b32 v106, a99\n\t"
      "v_mul_f32 v106, v106, v105\n\t"
      "v_accvgpr_write_b32 a99, v106\n\t"
      "v_accvgpr_read_b32 v106, a100\n\t"
      "v_mul_f32 v106, v106, v105\n\t"
      "v_accvgpr_write_b32 a100, v106\n\t"
      "v_accvgpr_read_b32 v106, a101\n\t"
      "v_mul_f32 v106, v106, v105\n\t"
      "v_accvgpr_write_b32 a101, v106\n\t"
      "v_accvgpr_read_b32 v106, a102\n\t"
      "v_mul_f32 v106, v106, v105\n\t"
      "v_accvgpr_write_b32 a102, v106\n\t"
      "v_accvgpr_read_b32 v106, a103\n\t"
      "v_mul_f32 v106, v106, v105\n\t"
      "v_accvgpr_write_b32 a103, v106\n\t"
      "v_accvgpr_read_b32 v106, a104\n\t"
      "v_mul_f32 v106, v106, v105\n\t"
      "v_accvgpr_write_b32 a104, v106\n\t"
      "s_branch L_back06_%=\n\t"
      "L_rare07_%=:\n\t"
      "s_nop 15\n\t"
      "v_mov_b32 v105, v104\n\t"
      "s_nop 1\n\t"
      "v_permlane32_swap_b32 v104, v105\n\t"
      "v_max3_f32 v104, v104, v105, 0\n\t"
      "v_exp_f32 v105, -v104\n\t"
      "v_sub_f32 v80, v80, v104\n\t"
      "v_sub_f32 v240, v240, v104\n\t"
      "v_sub_f32 v81, v81, v104\n\t"
      "v_sub_f32 v241, v241, v104\n\t"
      "v_sub_f32 v82, v82, v104\n\t"
      "v_sub_f32 v242, v242, v104\n\t"
      "v_sub_f32 v83, v83, v104\n\t"
      "v_sub_f32 v243, v243, v104\n\t"
      "v_sub_f32 v84, v84, v104\n\t"
      "v_sub_f32 v244, v244, v104\n\t"
      "v_sub_f32 v85, v85, v104\n\t"
      "v_sub_f32 v245, v245, v104\n\t"
      "v_sub_f32 v86, v86, v104\n\t"
      "v_sub_f32 v246, v246, v104\n\t"
      "v_sub_f32 v87, v87, v104\n\t"
      "v_sub_f32 v247, v247, v104\n\t"
      "v_sub_f32 v88, v88, v104\n\t"
      "v_sub_f32 v248, v248, v104\n\t"
      "v_sub_f32 v89, v89, v104\n\t"
      "v_sub_f32 v249, v249, v104\n\t"
      "v_sub_f32 v90, v90, v104\n\t"
      "v_sub_f32 v250, v250, v104\n\t"
      "v_sub_f32 v91, v91, v104\n\t"
      "v_sub_f32 v251, v251, v104\n\t"
      "v_sub_f32 v92, v92, v104\n\t"
      "v_sub_f32 v252, v252, v104\n\t"
      "v_sub_f32 v93, v93, v104\n\t"
      "v_sub_f32 v253, v253, v104\n\t"
      "v_sub_f32 v94, v94, v104\n\t"
      "v_sub_f32 v254, v254, v104\n\t"
      "v_sub_f32 v95, v95, v104\n\t"
      "v_sub_f32 v255, v255, v104\n\t"
      "v_accvgpr_read_b32 v106, a112\n\t"
      "v_mul_f32 v106, v106, v105\n\t"
      "v_accvgpr_write_b32 a112, v106\n\t"
      "v_accvgpr_read_b32 v106, a113\n\t"
      "v_mul_f32 v106, v106, v105\n\t"
      "v_accvgpr_write_b32 a113, v106\n\t"
      "v_accvgpr_read_b32 v106, a114\n\t"
      "v_mul_f32 v106, v106, v105\n\t"
      "v_accvgpr_write_b32 a114, v106\n\t"
      "v_accvgpr_read_b32 v106, a115\n\t"
      "v_mul_f32 v106, v106, v105\n\t"
      "v_accvgpr_write_b32 a115, v106\n\t"
      "v_accvgpr_read_b32 v106, a116\n\t"
      "v_mul_f32 v106, v106, v105\n\t"
      "v_accvgpr_write_b32 a116, v106\n\t"
      "v_accvgpr_read_b32 v106, a117\n\t"
      "v_mul_f32 v106, v106, v105\n\t"
      "v_accvgpr_write_b32 a117, v106\n\t"
      "v_accvgpr_read_b32 v106, a118\n\t"
      "v_mul_f32 v106, v106, v105\n\t"
      "v_accvgpr_write_b32 a118, v106\n\t"
      "v_accvgpr_read_b32 v106, a119\n\t"
      "v_mul_f32 v106, v106, v105\n\t"
      "v_accvgpr_write_b32 a119, v106\n\t"
      "v_accvgpr_read_b32 v106, a120\n\t"
      "v_mul_f32 v106, v106, v105\n\t"
      "v_accvgpr_write_b32 a120, v106\n\t"
      "s_branch L_back07_%=\n\t"
      "L_rare10_%=:\n\t"
      "s_nop 15\n\t"
      "v_mov_b32 v105, v104\n\t"
      "s_nop 1\n\t"
      "v_permlane32_swap_b32 v104, v105\n\t"
      "v_max3_f32 v104, v104, v105, 0\n\t"
      "v_exp_f32 v105, -v104\n\t"
      "v_sub_f32 v64, v64, v104\n\t"
      "v_sub_f32 v128, v128, v104\n\t"
      "v_sub_f32 v65, v65, v104\n\t"
      "v_sub_f32 v129, v129, v104\n\t"
      "v_sub_f32 v66, v66, v104\n\t"
      "v_sub_f32 v130, v130, v104\n\t"
      "v_sub_f32 v67, v67, v104\n\t"
      "v_sub_f32 v131, v131, v104\n\t"
      "v_sub_f32 v68, v68, v104\n\t"
      "v_sub_f32 v132, v132, v104\n\t"
      "v_sub_f32 v69, v69, v104\n\t"
      "v_sub_f32 v133, v133, v104\n\t"
      "v_sub_f32 v70, v70, v104\n\t"
      "v_sub_f32 v134, v134, v104\n\t"
      "v_sub_f32 v71, v71, v104\n\t"
      "v_sub_f32 v135, v135, v104\n\t"
      "v_sub_f32 v72, v72, v104\n\t"
      "v_sub_f32 v136, v136, v104\n\t"
      "v_sub_f32 v73, v73, v104\n\t"
      "v_sub_f32 v137, v137, v104\n\t"
      "v_sub_f32 v74, v74, v104\n\t"
      "v_sub_f32 v138, v138, v104\n\t"
      "v_sub_f32 v75, v75, v104\n\t"
      "v_sub_f32 v139, v139, v104\n\t"
      "v_sub_f32 v76, v76, v104\n\t"
      "v_sub_f32 v140, v140, v104\n\t"
      "v_sub_f32 v77, v77, v104\n\t"
      "v_sub_f32 v141, v141, v104\n\t"
      "v_sub_f32 v78, v78, v104\n\t"
      "v_sub_f32 v142, v142, v104\n\t"
      "v_sub_f32 v79, v79, v104\n\t"
      "v_sub_f32 v143, v143, v104\n\t"
      "v_accvgpr_read_b32 v106, a0\n\t"
      "v_mul_f32 v106, v106, v105\n\t"
      "v_accvgpr_write_b32 a0, v106\n\t"
      "v_accvgpr_read_b32 v106, a1\n\t"
      "v_mul_f32 v106, v106, v105\n\t"
      "v_accvgpr_write_b32 a1, v106\n\t"
      "v_accvgpr_read_b32 v106, a2\n\t"
      "v_mul_f32 v106, v106, v105\n\t"
      "v_accvgpr_write_b32 a2, v106\n\t"
      "v_accvgpr_read_b32 v106, a3\n\t"
      "v_mul_f32 v106, v106, v105\n\t"
      "v_accvgpr_write_b32 a3, v106\n\t"
      "v_accvgpr_read_b32 v106, a4\n\t"
      "v_mul_f32 v106, v106, v105\n\t"
      "v_accvgpr_write_b32 a4, v106\n\t"
      "v_accvgpr_read_b32 v106, a5\n\t"
      "v_mul_f32 v106, v106, v105\n\t"
      "v_accvgpr_write_b32 a5, v106\n\t"
      "v_accvgpr_read_b32 v106, a6\n\t"
      "v_mul_f32 v106, v106, v105\n\t"
      "v_accvgpr_write_b32 a6, v106\n\t"
      "v_accvgpr_read_b32 v106, a7\n\t"
      "v_mul_f32 v106, v106, v105\n\t"
      "v_accvgpr_write_b32 a7, v106\n\t"
      "v_accvgpr_read_b32 v106, a8\n\t"
      "v_mul_f32 v106, v106, v105\n\t"
      "v_accvgpr_write_b32 a8, v106\n\t"
      "s_branch L_back10_%=\n\t"
      "L_rare11_%=:\n\t"
      "s_nop 15\n\t"
      "v_mov_b32 v105, v104\n\t"
      "s_nop 1\n\t"
      "v_permlane32_swap_b32 v104, v105\n\t"
      "v_max3_f32 v104, v104, v105, 0\n\t"
      "v_exp_f32 v105, -v104\n\t"
      "v_sub_f32 v80, v80, v104\n\t"
      "v_sub_f32 v144, v144, v104\n\t"
      "v_sub_f32 v81, v81, v104\n\t"
      "v_sub_f32 v145, v145, v104\n\t"
      "v_sub_f32 v82, v82, v104\n\t"
      "v_sub_f32 v146, v146, v104\n\t"
      "v_sub_f32 v83, v83, v104\n\t"
      "v_sub_f32 v147, v147, v104\n\t"
      "v_sub_f32 v84, v84, v104\n\t"
      "v_sub_f32 v148, v148, v104\n\t"
      "v_sub_f32 v85, v85, v104\n\t"
      "v_sub_f32 v149, v149, v104\n\t"
      "v_sub_f32 v86, v86, v104\n\t"
      "v_sub_f32 v150, v150, v104\n\t"
      "v_sub_f32 v87, v87, v104\n\t"
      "v_sub_f32 v151, v151, v104\n\t"
      "v_sub_f32 v88, v88, v104\n\t"
      "v_sub_f32 v152, v152, v104\n\t"
      "v_sub_f32 v89, v89, v104\n\t"
      "v_sub_f32 v153, v153, v104\n\t"
      "v_sub_f32 v90, v90, v104\n\t"
      "v_sub_f32 v154, v154, v104\n\t"
      "v_sub_f32 v91, v91, v104\n\t"
      "v_sub_f32 v155, v155, v104\n\t"
      "v_sub_f32 v92, v92, v104\n\t"
      "v_sub_f32 v156, v156, v104\n\t"
      "v_sub_f32 v93, v93, v104\n\t"
      "v_sub_f32 v157, v157, v104\n\t"
      "v_sub_f32 v94, v94, v104\n\t"
      "v_sub_f32 v158, v158, v104\n\t"
      "v_sub_f32 v95, v95, v104\n\t"
      "v_sub_f32 v159, v159, v104\n\t"
      "v_accvgpr_read_b32 v106, a16\n\t"
      "v_mul_f32 v106, v106, v105\n\t"
      "v_accvgpr_write_b32 a16, v106\n\t"
      "v_accvgpr_read_b32 v106, a17\n\t"
      "v_mul_f32 v106, v106, v105\n\t"
      "v_accvgpr_write_b32 a17, v106\n\t"
      "v_accvgpr_read_b32 v106, a18\n\t"
      "v_mul_f32 v106, v106, v105\n\t"
      "v_accvgpr_write_b32 a18, v106\n\t"
      "v_accvgpr_read_b32 v106, a19\n\t"
      "v_mul_f32 v106, v106, v105\n\t"
      "v_accvgpr_write_b32 a19, v106\n\t"
      "v_accvgpr_read_b32 v106, a20\n\t"
      "v_mul_f32 v106, v106, v105\n\t"
      "v_accvgpr_write_b32 a20, v106\n\t"
      "v_accvgpr_read_b32 v106, a21\n\t"
      "v_mul_f32 v106, v106, v105\n\t"
      "v_accvgpr_write_b32 a21, v106\n\t"
      "v_accvgpr_read_b32 v106, a22\n\t"
      "v_mul_f32 v106, v106, v105\n\t"
      "v_accvgpr_write_b32 a22, v106\n\t"
      "v_accvgpr_read_b32 v106, a23\n\t"
      "v_mul_f32 v106, v106, v105\n\t"
      "v_accvgpr_write_b32 a23, v106\n\t"
      "v_accvgpr_read_b32 v106, a24\n\t"
      "v_mul_f32 v106, v106, v105\n\t"
      "v_accvgpr_write_b32 a24, v106\n\t"
      "s_branch L_back11_%=\n\t"
      "L_rare12_%=:\n\t"
      "s_nop 15\n\t"
      "v_mov_b32 v105, v104\n\t"
      "s_nop 1\n\t"
      "v_permlane32_swap_b32 v104, v105\n\t"
      "v_max3_f32 v104, v104, v105, 0\n\t"
      "v_exp_f32 v105, -v104\n\t"
      "v_sub_f32 v64, v64, v104\n\t"
      "v_sub_f32 v160, v160, v104\n\t"
      "v_sub_f32 v65, v65, v104\n\t"
      "v_sub_f32 v161, v161, v104\n\t"
      "v_sub_f32 v66, v66, v104\n\t"
      "v_sub_f32 v162, v162, v104\n\t"
      "v_sub_f32 v67, v67, v104\n\t"
      "v_sub_f32 v163, v163, v104\n\t"
      "v_sub_f32 v68, v68, v104\n\t"
      "v_sub_f32 v164, v164, v104\n\t"
      "v_sub_f32 v69, v69, v104\n\t"
      "v_sub_f32 v165, v165, v104\n\t"
      "v_sub_f32 v70, v70, v104\n\t"
      "v_sub_f32 v166, v166, v104\n\t"
      "v_sub_f32 v71, v71, v104\n\t"
      "v_sub_f32 v167, v167, v104\n\t"
      "v_sub_f32 v72, v72, v104\n\t"
      "v_sub_f32 v168, v168, v104\n\t"
      "v_sub_f32 v73, v73, v104\n\t"
      "v_sub_f32 v169, v169, v104\n\t"
      "v_sub_f32 v74, v74, v104\n\t"
      "v_sub_f32 v170, v170, v104\n\t"
      "v_sub_f32 v75, v75, v104\n\t"
      "v_sub_f32 v171, v171, v104\n\t"
      "v_sub_f32 v76, v76, v104\n\t"
      "v_sub_f32 v172, v172, v104\n\t"
      "v_sub_f32 v77, v77, v104\n\t"
      "v_sub_f32 v173, v173, v104\n\t"
      "v_sub_f32 v78, v78, v104\n\t"
      "v_sub_f32 v174, v174, v104\n\t"
      "v_sub_f32 v79, v79, v104\n\t"
      "v_sub_f32 v175, v175, v104\n\t"
      "v_accvgpr_read_b32 v106, a32\n\t"
      "v_mul_f32 v106, v106, v105\n\t"
      "v_accvgpr_write_b32 a32, v106\n\t"
      "v_accvgpr_read_b32 v106, a33\n\t"
      "v_mul_f32 v106, v106, v105\n\t"
      "v_accvgpr_write_b32 a33, v106\n\t"
      "v_accvgpr_read_b32 v106, a34\n\t"
      "v_mul_f32 v106, v106, v105\n\t"
      "v_accvgpr_write_b32 a34, v106\n\t"
      "v_accvgpr_read_b32 v106, a35\n\t"
      "v_mul_f32 v106, v106, v105\n\t"
      "v_accvgpr_write_b32 a35, v106\n\t"
      "v_accvgpr_read_b32 v106, a36\n\t"
      "v_mul_f32 v106, v106, v105\n\t"
      "v_accvgpr_write_b32 a36, v106\n\t"
      "v_accvgpr_read_b32 v106, a37\n\t"
      "v_mul_f32 v106, v106, v105\n\t"
      "v_accvgpr_write_b32 a37, v106\n\t"
      "v_accvgpr_read_b32 v106, a38\n\t"
      "v_mul_f32 v106, v106, v105\n\t"
      "v_accvgpr_write_b32 a38, v106\n\t"
      "v_accvgpr_read_b32 v106, a39\n\t"
      "v_mul_f32 v106, v106, v105\n\t"
      "v_accvgpr_write_b32 a39, v106\n\t"
      "v_accvgpr_read_b32 v106, a40\n\t"
      "v_mul_f32 v106, v106, v105\n\t"
      "v_accvgpr_write_b32 a40, v106\n\t"
      "s_branch L_back12_%=\n\t"
      "L_rare13_%=:\n\t"
      "s_nop 15\n\t"
      "v_mov_b32 v105, v104\n\t"
      "s_nop 1\n\t"
      "v_permlane32_swap_b32 v104, v105\n\t"
      "v_max3_f32 v104, v104, v105, 0\n\t"
      "v_exp_f32 v105, -v104\n\t"
      "v_sub_f32 v80, v80, v104\n\t"
      "v_sub_f32 v176, v176, v104\n\t"
      "v_sub_f32 v81, v81, v104\n\t"
      "v_sub_f32 v177, v177, v104\n\t"
      "v_sub_f32 v82, v82, v104\n\t"
      "v_sub_f32 v178, v178, v104\n\t"
      "v_sub_f32 v83, v83, v104\n\t"
      "v_sub_f32 v179, v179, v104\n\t"
      "v_sub_f32 v84, v84, v104\n\t"
      "v_sub_f32 v180, v180, v104\n\t"
      "v_sub_f32 v85, v85, v104\n\t"
      "v_sub_f32 v181, v181, v104\n\t"
      "v_sub_f32 v86, v86, v104\n\t"
      "v_sub_f32 v182, v182, v104\n\t"
      "v_sub_f32 v87, v87, v104\n\t"
      "v_sub_f32 v183, v183, v104\n\t"
      "v_sub_f32 v88, v88, v104\n\t"
      "v_sub_f32 v184, v184, v104\n\t"
      "v_sub_f32 v89, v89, v104\n\t"
      "v_sub_f32 v185, v185, v104\n\t"
      "v_sub_f32 v90, v90, v104\n\t"
      "v_sub_f32 v186, v186, v104\n\t"
      "v_sub_f32 v91, v91, v104\n\t"
      "v_sub_f32 v187, v187, v104\n\t"
      "v_sub_f32 v92, v92, v104\n\t"
      "v_sub_f32 v188, v188, v104\n\t"
      "v_sub_f32 v93, v93, v104\n\t"
      "v_sub_f32 v189, v189, v104\n\t"
      "v_sub_f32 v94, v94, v104\n\t"
      "v_sub_f32 v190, v190, v104\n\t"
      "v_sub_f32 v95, v95, v104\n\t"
      "v_sub_f32 v191, v191, v104\n\t"
      "v_accvgpr_read_b32 v106, a48\n\t"
      "v_mul_f32 v106, v106, v105\n\t"
      "v_accvgpr_write_b32 a48, v106\n\t"
      "v_accvgpr_read_b32 v106, a49\n\t"
      "v_mul_f32 v106, v106, v105\n\t"
      "v_accvgpr_write_b32 a49, v106\n\t"
      "v_accvgpr_read_b32 v106, a50\n\t"
      "v_mul_f32 v106, v106, v105\n\t"
      "v_accvgpr_write_b32 a50, v106\n\t"
      "v_accvgpr_read_b32 v106, a51\n\t"
      "v_mul_f32 v106, v106, v105\n\t"
      "v_accvgpr_write_b32 a51, v106\n\t"
      "v_accvgpr_read_b32 v106, a52\n\t"
      "v_mul_f32 v106, v106, v105\n\t"
      "v_accvgpr_write_b32 a52, v106\n\t"
      "v_accvgpr_read_b32 v106, a53\n\t"
      "v_mul_f32 v106, v106, v105\n\t"
      "v_accvgpr_write_b32 a53, v106\n\t"
      "v_accvgpr_read_b32 v106, a54\n\t"
      "v_mul_f32 v106, v106, v105\n\t"
      "v_accvgpr_write_b32 a54, v106\n\t"
      "v_accvgpr_read_b32 v106, a55\n\t"
      "v_mul_f32 v106, v106, v105\n\t"
      "v_accvgpr_write_b32 a55, v106\n\t"
      "v_accvgpr_read_b32 v106, a56\n\t"
      "v_mul_f32 v106, v106, v105\n\t"
      "v_accvgpr_write_b32 a56, v106\n\t"
      "s_branch L_back13_%=\n\t"
      "L_rare14_%=:\n\t"
      "s_nop 15\n\t"
      "v_mov_b32 v105, v104\n\t"
      "s_nop 1\n\t"
      "v_permlane32_swap_b32 v104, v105\n\t"
      "v_max3_f32 v104, v104, v105, 0\n\t"
      "v_exp_f32 v105, -v104\n\t"
      "v_sub_f32 v64, v64, v104\n\t"
      "v_sub_f32 v192, v192, v104\n\t"
      "v_sub_f32 v65, v65, v104\n\t"
      "v_sub_f32 v193, v193, v104\n\t"
      "v_sub_f32 v66, v66, v104\n\t"
      "v_sub_f32 v194, v194, v104\n\t"
      "v_sub_f32 v67, v67, v104\n\t"
      "v_sub_f32 v195, v195, v104\n\t"
      "v_sub_f32 v68, v68, v104\n\t"
      "v_sub_f32 v196, v196, v104\n\t"
      "v_sub_f32 v69, v69, v104\n\t"
      "v_sub_f32 v197, v197, v104\n\t"
      "v_sub_f32 v70, v70, v104\n\t"
      "v_sub_f32 v198, v198, v104\n\t"
      "v_sub_f32 v71, v71, v104\n\t"
      "v_sub_f32 v199, v199, v104\n\t"
      "v_sub_f32 v72, v72, v104\n\t"
      "v_sub_f32 v200, v200, v104\n\t"
      "v_sub_f32 v73, v73, v104\n\t"
      "v_sub_f32 v201, v201, v104\n\t"
      "v_sub_f32 v74, v74, v104\n\t"
      "v_sub_f32 v202, v202, v104\n\t"
      "v_sub_f32 v75, v75, v104\n\t"
      "v_sub_f32 v203, v203, v104\n\t"
      "v_sub_f32 v76, v76, v104\n\t"
      "v_sub_f32 v204, v204, v104\n\t"
      "v_sub_f32 v77, v77, v104\n\t"
      "v_sub_f32 v205, v205, v104\n\t"
      "v_sub_f32 v78, v78, v104\n\t"
      "v_sub_f32 v206, v206, v104\n\t"
      "v_sub_f32 v79, v79, v104\n\t"
      "v_sub_f32 v207, v207, v104\n\t"
      "v_accvgpr_read_b32 v106, a64\n\t"
      "v_mul_f32 v106, v106, v105\n\t"
      "v_accvgpr_write_b32 a64, v106\n\t"
      "v_accvgpr_read_b32 v106, a65\n\t"
      "v_mul_f32 v106, v106, v105\n\t"
      "v_accvgpr_write_b32 a65, v106\n\t"
      "v_accvgpr_read_b32 v106, a66\n\t"
      "v_mul_f32 v106, v106, v105\n\t"
      "v_accvgpr_write_b32 a66, v106\n\t"
      "v_accvgpr_read_b32 v106, a67\n\t"
      "v_mul_f32 v106, v106, v105\n\t"
      "v_accvgpr_write_b32 a67, v106\n\t"
      "v_accvgpr_read_b32 v106, a68\n\t"
      "v_mul_f32 v106, v106, v105\n\t"
      "v_accvgpr_write_b32 a68, v106\n\t"
      "v_accvgpr_read_b32 v106, a69\n\t"
      "v_mul_f32 v106, v106, v105\n\t"
      "v_accvgpr_write_b32 a69, v106\n\t"
      "v_accvgpr_read_b32 v106, a70\n\t"
      "v_mul_f32 v106, v106, v105\n\t"
      "v_accvgpr_write_b32 a70, v106\n\t"
      "v_accvgpr_read_b32 v106, a71\n\t"
      "v_mul_f32 v106, v106, v105\n\t"
      "v_accvgpr_write_b32 a71, v106\n\t"
      "v_accvgpr_read_b32 v106, a72\n\t"
      "v_mul_f32 v106, v106, v105\n\t"
      "v_accvgpr_write_b32 a72, v106\n\t"
      "s_branch L_back14_%=\n\t"
      "L_rare15_%=:\n\t"
      "s_nop 15\n\t"
      "v_mov_b32 v105, v104\n\t"
      "s_nop 1\n\t"
      "v_permlane32_swap_b32 v104, v105\n\t"
      "v_max3_f32 v104, v104, v105, 0\n\t"
      "v_exp_f32 v105, -v104\n\t"
      "v_sub_f32 v80, v80, v104\n\t"
      "v_sub_f32 v208, v208, v104\n\t"
      "v_sub_f32 v81, v81, v104\n\t"
      "v_sub_f32 v209, v209, v104\n\t"
      "v_sub_f32 v82, v82, v104\n\t"
      "v_sub_f32 v210, v210, v104\n\t"
      "v_sub_f32 v83, v83, v104\n\t"
      "v_sub_f32 v211, v211, v104\n\t"
      "v_sub_f32 v84, v84, v104\n\t"
      "v_sub_f32 v212, v212, v104\n\t"
      "v_sub_f32 v85, v85, v104\n\t"
      "v_sub_f32 v213, v213, v104\n\t"
      "v_sub_f32 v86, v86, v104\n\t"
      "v_sub_f32 v214, v214, v104\n\t"
      "v_sub_f32 v87, v87, v104\n\t"
      "v_sub_f32 v215, v215, v104\n\t"
      "v_sub_f32 v88, v88, v104\n\t"
      "v_sub_f32 v216, v216, v104\n\t"
      "v_sub_f32 v89, v89, v104\n\t"
      "v_sub_f32 v217, v217, v104\n\t"
      "v_sub_f32 v90, v90, v104\n\t"
      "v_sub_f32 v218, v218, v104\n\t"
      "v_sub_f32 v91, v91, v104\n\t"
      "v_sub_f32 v219, v219, v104\n\t"
      "v_sub_f32 v92, v92, v104\n\t"
      "v_sub_f32 v220, v220, v104\n\t"
      "v_sub_f32 v93, v93, v104\n\t"
      "v_sub_f32 v221, v221, v104\n\t"
      "v_sub_f32 v94, v94, v104\n\t"
      "v_sub_f32 v222, v222, v104\n\t"
      "v_sub_f32 v95, v95, v104\n\t"
      "v_sub_f32 v223, v223, v104\n\t"
      "v_accvgpr_read_b32 v106, a80\n\t"
      "v_mul_f32 v106, v106, v105\n\t"
      "v_accvgpr_write_b32 a80, v106\n\t"
      "v_accvgpr_read_b32 v106, a81\n\t"
      "v_mul_f32 v106, v106, v105\n\t"
      "v_accvgpr_write_b32 a81, v106\n\t"
      "v_accvgpr_read_b32 v106, a82\n\t"
      "v_mul_f32 v106, v106, v105\n\t"
      "v_accvgpr_write_b32 a82, v106\n\t"
      "v_accvgpr_read_b32 v106, a83\n\t"
      "v_mul_f32 v106, v106, v105\n\t"
      "v_accvgpr_write_b32 a83, v106\n\t"
      "v_accvgpr_read_b32 v106, a84\n\t"
      "v_mul_f32 v106, v106, v105\n\t"
      "v_accvgpr_write_b32 a84, v106\n\t"
      "v_accvgpr_read_b32 v106, a85\n\t"
      "v_mul_f32 v106, v106, v105\n\t"
      "v_accvgpr_write_b32 a85, v106\n\t"
      "v_accvgpr_read_b32 v106, a86\n\t"
      "v_mul_f32 v106, v106, v105\n\t"
      "v_accvgpr_write_b32 a86, v106\n\t"
      "v_accvgpr_read_b32 v106, a87\n\t"
      "v_mul_f32 v106, v106, v105\n\t"
      "v_accvgpr_write_b32 a87, v106\n\t"
      "v_accvgpr_read_b32 v106, a88\n\t"
      "v_mul_f32 v106, v106, v105\n\t"
      "v_accvgpr_write_b32 a88, v106\n\t"
      "s_branch L_back15_%=\n\t"
      "L_rare16_%=:\n\t"
      "s_nop 15\n\t"
      "v_mov_b32 v105, v104\n\t"
      "s_nop 1\n\t"
      "v_permlane32_swap_b32 v104, v105\n\t"
      "v_max3_f32 v104, v104, v105, 0\n\t"
      "v_exp_f32 v105, -v104\n\t"
      "v_sub_f32 v64, v64, v104\n\t"
      "v_sub_f32 v224, v224, v104\n\t"
      "v_sub_f32 v65, v65, v104\n\t"
      "v_sub_f32 v225, v225, v104\n\t"
      "v_sub_f32 v66, v66, v104\n\t"
      "v_sub_f32 v226, v226, v104\n\t"
      "v_sub_f32 v67, v67, v104\n\t"
      "v_sub_f32 v227, v227, v104\n\t"
      "v_sub_f32 v68, v68, v104\n\t"
      "v_sub_f32 v228, v228, v104\n\t"
      "v_sub_f32 v69, v69, v104\n\t"
      "v_sub_f32 v229, v229, v104\n\t"
      "v_sub_f32 v70, v70, v104\n\t"
      "v_sub_f32 v230, v230, v104\n\t"
      "v_sub_f32 v71, v71, v104\n\t"
      "v_sub_f32 v231, v231, v104\n\t"
      "v_sub_f32 v72, v72, v104\n\t"
      "v_sub_f32 v232, v232, v104\n\t"
      "v_sub_f32 v73, v73, v104\n\t"
      "v_sub_f32 v233, v233, v104\n\t"
      "v_sub_f32 v74, v74, v104\n\t"
      "v_sub_f32 v234, v234, v104\n\t"
      "v_sub_f32 v75, v75, v104\n\t"
      "v_sub_f32 v235, v235, v104\n\t"
      "v_sub_f32 v76, v76, v104\n\t"
      "v_sub_f32 v236, v236, v104\n\t"
      "v_sub_f32 v77, v77, v104\n\t"
      "v_sub_f32 v237, v237, v104\n\t"
      "v_sub_f32 v78, v78, v104\n\t"
      "v_sub_f32 v238, v238, v104\n\t"
      "v_sub_f32 v79, v79, v104\n\t"
      "v_sub_f32 v239, v239, v104\n\t"
      "v_accvgpr_read_b32 v106, a96\n\t"
      "v_mul_f32 v106, v106, v105\n\t"
      "v_accvgpr_write_b32 a96, v106\n\t"
      "v_accvgpr_read_b32 v106, a97\n\t"
      "v_mul_f32 v106, v106, v105\n\t"
      "v_accvgpr_write_b32 a97, v106\n\t"
      "v_accvgpr_read_b32 v106, a98\n\t"
      "v_mul_f32 v106, v106, v105\n\t"
      "v_accvgpr_write_b32 a98, v106\n\t"
      "v_accvgpr_read_b32 v106, a99\n\t"
      "v_mul_f32 v106, v106, v105\n\t"
      "v_accvgpr_write_b32 a99, v106\n\t"
      "v_accvgpr_read_b32 v106, a100\n\t"
      "v_mul_f32 v106, v106, v105\n\t"
      "v_accvgpr_write_b32 a100, v106\n\t"
      "v_accvgpr_read_b32 v106, a101\n\t"
      "v_mul_f32 v106, v106, v105\n\t"
      "v_accvgpr_write_b32 a101, v106\n\t"
      "v_accvgpr_read_b32 v106, a102\n\t"
      "v_mul_f32 v106, v106, v105\n\t"
      "v_accvgpr_write_b32 a102, v106\n\t"
      "v_accvgpr_read_b32 v106, a103\n\t"
      "v_mul_f32 v106, v106, v105\n\t"
      "v_accvgpr_write_b32 a103, v106\n\t"
      "v_accvgpr_read_b32 v106, a104\n\t"
      "v_mul_f32 v106, v106, v105\n\t"
      "v_accvgpr_write_b32 a104, v106\n\t"
      "s_branch L_back16_%=\n\t"
      "L_rare17_%=:\n\t"
      "s_nop 15\n\t"
      "v_mov_b32 v105, v104\n\t"
      "s_nop 1\n\t"
      "v_permlane32_swap_b32 v104, v105\n\t"
      "v_max3_f32 v104, v104, v105, 0\n\t"
      "v_exp_f32 v105, -v104\n\t"
      "v_sub_f32 v80, v80, v104\n\t"
      "v_sub_f32 v240, v240, v104\n\t"
      "v_sub_f32 v81, v81, v104\n\t"
      "v_sub_f32 v241, v241, v104\n\t"
      "v_sub_f32 v82, v82, v104\n\t"
      "v_sub_f32 v242, v242, v104\n\t"
      "v_sub_f32 v83, v83, v104\n\t"
      "v_sub_f32 v243, v243, v104\n\t"
      "v_sub_f32 v84, v84, v104\n\t"
      "v_sub_f32 v244, v244, v104\n\t"
      "v_sub_f32 v85, v85, v104\n\t"
      "v_sub_f32 v245, v245, v104\n\t"
      "v_sub_f32 v86, v86, v104\n\t"
      "v_sub_f32 v246, v246, v104\n\t"
      "v_sub_f32 v87, v87, v104\n\t"
      "v_sub_f32 v247, v247, v104\n\t"
      "v_sub_f32 v88, v88, v104\n\t"
      "v_sub_f32 v248, v248, v104\n\t"
      "v_sub_f32 v89, v89, v104\n\t"
      "v_sub_f32 v249, v249, v104\n\t"
      "v_sub_f32 v90, v90, v104\n\t"
      "v_sub_f32 v250, v250, v104\n\t"
      "v_sub_f32 v91, v91, v104\n\t"
      "v_sub_f32 v251, v251, v104\n\t"
      "v_sub_f32 v92, v92, v104\n\t"
      "v_sub_f32 v252, v252, v104\n\t"
      "v_sub_f32 v93, v93, v104\n\t"
      "v_sub_f32 v253, v253, v104\n\t"
      "v_sub_f32 v94, v94, v104\n\t"
      "v_sub_f32 v254, v254, v104\n\t"
      "v_sub_f32 v95, v95, v104\n\t"
      "v_sub_f32 v255, v255, v104\n\t"
      "v_accvgpr_read_b32 v106, a112\n\t"
      "v_mul_f32 v106, v106, v105\n\t"
      "v_accvgpr_write_b32 a112, v106\n\t"
      "v_accvgpr_read_b32 v106, a113\n\t"
      "v_mul_f32 v106, v106, v105\n\t"
      "v_accvgpr_write_b32 a113, v106\n\t"
      "v_accvgpr_read_b32 v106, a114\n\t"
      "v_mul_f32 v106, v106, v105\n\t"
      "v_accvgpr_write_b32 a114, v106\n\t"
      "v_accvgpr_read_b32 v106, a115\n\t"
      "v_mul_f32 v106, v106, v105\n\t"
      "v_accvgpr_write_b32 a115, v106\n\t"
      "v_accvgpr_read_b32 v106, a116\n\t"
      "v_mul_f32 v106, v106, v105\n\t"
      "v_accvgpr_write_b32 a116, v106\n\t"
      "v_accvgpr_read_b32 v106, a117\n\t"
      "v_mul_f32 v106, v106, v105\n\t"
      "v_accvgpr_write_b32 a117, v106\n\t"
      "v_accvgpr_read_b32 v106, a118\n\t"
      "v_mul_f32 v106, v106, v105\n\t"
      "v_accvgpr_write_b32 a118, v106\n\t"
      "v_accvgpr_read_b32 v106, a119\n\t"
      "v_mul_f32 v106, v106, v105\n\t"
      "v_accvgpr_write_b32 a119, v106\n\t"
      "v_accvgpr_read_b32 v106, a120\n\t"
      "v_mul_f32 v106, v106, v105\n\t"
      "v_accvgpr_write_b32 a120, v106\n\t"
      "s_branch L_back17_%=\n\t"
      "L_fin_%=:\n\t"
      "s_nop 15\n\t"
      "v_accvgpr_read_b32 v104, a8\n\t"
      "v_mov_b32 v105, v104\n\t"
      "s_nop 1\n\t"
      "v_permlane32_swap_b32 v104, v105\n\t"
      "v_rcp_f32 v104, v104\n\t"
      "v_accvgpr_read_b32 v64, a0\n\t"
      "v_accvgpr_read_b32 v65, a1\n\t"
      "v_accvgpr_read_b32 v66, a2\n\t"
      "v_accvgpr_read_b32 v67, a3\n\t"
      "v_accvgpr_read_b32 v68, a4\n\t"
      "v_accvgpr_read_b32 v69, a5\n\t"
      "v_accvgpr_read_b32 v70, a6\n\t"
      "v_accvgpr_read_b32 v71, a7\n\t"
      "v_mul_f32 v64, v64, v104\n\t"
      "v_mul_f32 v65, v65, v104\n\t"
      "v_mul_f32 v66, v66, v104\n\t"
      "v_mul_f32 v67, v67, v104\n\t"
      "v_mul_f32 v68, v68, v104\n\t"
      "v_mul_f32 v69, v69, v104\n\t"
      "v_mul_f32 v70, v70, v104\n\t"
      "v_mul_f32 v71, v71, v104\n\t"
      "v_cvt_pk_bf16_f32 v96, v64, v65\n\t"
      "v_cvt_pk_bf16_f32 v97, v66, v67\n\t"
      "v_cvt_pk_bf16_f32 v98, v68, v69\n\t"
      "v_cvt_pk_bf16_f32 v99, v70, v71\n\t"
      "s_add_u32 s42, %[rot], 0\n\t"
      "s_and_b32 s42, s42, 7\n\t"
      "s_lshl_b32 s42, s42, 17\n\t"
      "s_add_u32 s42, s42, %[so]\n\t"
      "buffer_store_dwordx4 v[96:99], %[ob], %[srd], s42 offen sc1\n\t"
      "v_accvgpr_read_b32 v104, a24\n\t"
      "v_mov_b32 v105, v104\n\t"
      "s_nop 1\n\t"
      "v_permlane32_swap_b32 v104, v105\n\t"
      "v_rcp_f32 v104, v104\n\t"
      "v_accvgpr_read_b32 v64, a16\n\t"
      "v_accvgpr_read_b32 v65, a17\n\t"
      "v_accvgpr_read_b32 v66, a18\n\t"
      "v_accvgpr_read_b32 v67, a19\n\t"
      "v_accvgpr_read_b32 v68, a20\n\t"
      "v_accvgpr_read_b32 v69, a21\n\t"
      "v_accvgpr_read_b32 v70, a22\n\t"
      "v_accvgpr_read_b32 v71, a23\n\t"
      "v_mul_f32 v64, v64, v104\n\t"
      "v_mul_f32 v65, v65, v104\n\t"
      "v_mul_f32 v66, v66, v104\n\t"
      "v_mul_f32 v67, v67, v104\n\t"
      "v_mul_f32 v68, v68, v104\n\t"
      "v_mul_f32 v69, v69, v104\n\t"
      "v_mul_f32 v70, v70, v104\n\t"
      "v_mul_f32 v71, v71, v104\n\t"
      "v_cvt_pk_bf16_f32 v96, v64, v65\n\t"
      "v_cvt_pk_bf16_f32 v97, v66, v67\n\t"
      "v_cvt_pk_bf16_f32 v98, v68, v69\n\t"
      "v_cvt_pk_bf16_f32 v99, v70, v71\n\t"
      "s_add_u32 s42, %[rot], 1\n\t"
      "s_and_b32 s42, s42, 7\n\t"
      "s_lshl_b32 s42, s42, 17\n\t"
      "s_add_u32 s42, s42, %[so]\n\t"
      "buffer_store_dwordx4 v[96:99], %[ob], %[srd], s42 offen sc1\n\t"
      "v_accvgpr_read_b32 v104, a40\n\t"
      "v_mov_b32 v105, v104\n\t"
      "s_nop 1\n\t"
      "v_permlane32_swap_b32 v104, v105\n\t"
      "v_rcp_f32 v104, v104\n\t"
      "v_accvgpr_read_b32 v64, a32\n\t"
      "v_accvgpr_read_b32 v65, a33\n\t"
      "v_accvgpr_read_b32 v66, a34\n\t"
      "v_accvgpr_read_b32 v67, a35\n\t"
      "v_accvgpr_read_b32 v68, a36\n\t"
      "v_accvgpr_read_b32 v69, a37\n\t"
      "v_accvgpr_read_b32 v70, a38\n\t"
      "v_accvgpr_read_b32 v71, a39\n\t"
      "v_mul_f32 v64, v64, v104\n\t"
      "v_mul_f32 v65, v65, v104\n\t"
      "v_mul_f32 v66, v66, v104\n\t"
      "v_mul_f32 v67, v67, v104\n\t"
      "v_mul_f32 v68, v68, v104\n\t"
      "v_mul_f32 v69, v69, v104\n\t"
      "v_mul_f32 v70, v70, v104\n\t"
      "v_mul_f32 v71, v71, v104\n\t"
      "v_cvt_pk_bf16_f32 v96, v64, v65\n\t"
      "v_cvt_pk_bf16_f32 v97, v66, v67\n\t"
      "v_cvt_pk_bf16_f32 v98, v68, v69\n\t"
      "v_cvt_pk_bf16_f32 v99, v70, v71\n\t"
      "s_add_u32 s42, %[rot], 2\n\t"
      "s_and_b32 s42, s42, 7\n\t"
      "s_lshl_b32 s42, s42, 17\n\t"
      "s_add_u32 s42, s42, %[so]\n\t"
      "buffer_store_dwordx4 v[96:99], %[ob], %[srd], s42 offen sc1\n\t"
      "v_accvgpr_read_b32 v104, a56\n\t"
      "v_mov_b32 v105, v104\n\t"
      "s_nop 1\n\t"
      "v_permlane32_swap_b32 v104, v105\n\t"
      "v_rcp_f32 v104, v104\n\t"
      "v_accvgpr_read_b32 v64, a48\n\t"
      "v_accvgpr_read_b32 v65, a49\n\t"
      "v_accvgpr_read_b32 v66, a50\n\t"
      "v_accvgpr_read_b32 v67, a51\n\t"
      "v_accvgpr_read_b32 v68, a52\n\t"
      "v_accvgpr_read_b32 v69, a53\n\t"
      "v_accvgpr_read_b32 v70, a54\n\t"
      "v_accvgpr_read_b32 v71, a55\n\t"
      "v_mul_f32 v64, v64, v104\n\t"
      "v_mul_f32 v65, v65, v104\n\t"
      "v_mul_f32 v66, v66, v104\n\t"
      "v_mul_f32 v67, v67, v104\n\t"
      "v_mul_f32 v68, v68, v104\n\t"
      "v_mul_f32 v69, v69, v104\n\t"
      "v_mul_f32 v70, v70, v104\n\t"
      "v_mul_f32 v71, v71, v104\n\t"
      "v_cvt_pk_bf16_f32 v96, v64, v65\n\t"
      "v_cvt_pk_bf16_f32 v97, v66, v67\n\t"
      "v_cvt_pk_bf16_f32 v98, v68, v69\n\t"
      "v_cvt_pk_bf16_f32 v99, v70, v71\n\t"
      "s_add_u32 s42, %[rot], 3\n\t"
      "s_and_b32 s42, s42, 7\n\t"
      "s_lshl_b32 s42, s42, 17\n\t"
      "s_add_u32 s42, s42, %[so]\n\t"
      "buffer_store_dwordx4 v[96:99], %[ob], %[srd], s42 offen sc1\n\t"
      "v_accvgpr_read_b32 v104, a72\n\t"
      "v_mov_b32 v105, v104\n\t"
      "s_nop 1\n\t"
      "v_permlane32_swap_b32 v104, v105\n\t"
      "v_rcp_f32 v104, v104\n\t"
      "v_accvgpr_read_b32 v64, a64\n\t"
      "v_accvgpr_read_b32 v65, a65\n\t"
      "v_accvgpr_read_b32 v66, a66\n\t"
      "v_accvgpr_read_b32 v67, a67\n\t"
      "v_accvgpr_read_b32 v68, a68\n\t"
      "v_accvgpr_read_b32 v69, a69\n\t"
      "v_accvgpr_read_b32 v70, a70\n\t"
      "v_accvgpr_read_b32 v71, a71\n\t"
      "v_mul_f32 v64, v64, v104\n\t"
      "v_mul_f32 v65, v65, v104\n\t"
      "v_mul_f32 v66, v66, v104\n\t"
      "v_mul_f32 v67, v67, v104\n\t"
      "v_mul_f32 v68, v68, v104\n\t"
      "v_mul_f32 v69, v69, v104\n\t"
      "v_mul_f32 v70, v70, v104\n\t"
      "v_mul_f32 v71, v71, v104\n\t"
      "v_cvt_pk_bf16_f32 v96, v64, v65\n\t"
      "v_cvt_pk_bf16_f32 v97, v66, v67\n\t"
      "v_cvt_pk_bf16_f32 v98, v68, v69\n\t"
      "v_cvt_pk_bf16_f32 v99, v70, v71\n\t"
      "s_add_u32 s42, %[rot], 4\n\t"
      "s_and_b32 s42, s42, 7\n\t"
      "s_lshl_b32 s42, s42, 17\n\t"
      "s_add_u32 s42, s42, %[so]\n\t"
      "buffer_store_dwordx4 v[96:99], %[ob], %[srd], s42 offen sc1\n\t"
      "v_accvgpr_read_b32 v104, a88\n\t"
      "v_mov_b32 v105, v104\n\t"
      "s_nop 1\n\t"
      "v_permlane32_swap_b32 v104, v105\n\t"
      "v_rcp_f32 v104, v104\n\t"
      "v_accvgpr_read_b32 v64, a80\n\t"
      "v_accvgpr_read_b32 v65, a81\n\t"
      "v_accvgpr_read_b32 v66, a82\n\t"
      "v_accvgpr_read_b32 v67, a83\n\t"
      "v_accvgpr_read_b32 v68, a84\n\t"
      "v_accvgpr_read_b32 v69, a85\n\t"
      "v_accvgpr_read_b32 v70, a86\n\t"
      "v_accvgpr_read_b32 v71, a87\n\t"
      "v_mul_f32 v64, v64, v104\n\t"
      "v_mul_f32 v65, v65, v104\n\t"
      "v_mul_f32 v66, v66, v104\n\t"
      "v_mul_f32 v67, v67, v104\n\t"
      "v_mul_f32 v68, v68, v104\n\t"
      "v_mul_f32 v69, v69, v104\n\t"
      "v_mul_f32 v70, v70, v104\n\t"
      "v_mul_f32 v71, v71, v104\n\t"
      "v_cvt_pk_bf16_f32 v96, v64, v65\n\t"
      "v_cvt_pk_bf16_f32 v97, v66, v67\n\t"
      "v_cvt_pk_bf16_f32 v98, v68, v69\n\t"
      "v_cvt_pk_bf16_f32 v99, v70, v71\n\t"
      "s_add_u32 s42, %[rot], 5\n\t"
      "s_and_b32 s42, s42, 7\n\t"
      "s_lshl_b32 s42, s42, 17\n\t"
      "s_add_u32 s42, s42, %[so]\n\t"
      "buffer_store_dwordx4 v[96:99], %[ob], %[srd], s42 offen sc1\n\t"
      "v_accvgpr_read_b32 v104, a104\n\t"
      "v_mov_b32 v105, v104\n\t"
      "s_nop 1\n\t"
      "v_permlane32_swap_b32 v104, v105\n\t"
      "v_rcp_f32 v104, v104\n\t"
      "v_accvgpr_read_b32 v64, a96\n\t"
      "v_accvgpr_read_b32 v65, a97\n\t"
      "v_accvgpr_read_b32 v66, a98\n\t"
      "v_accvgpr_read_b32 v67, a99\n\t"
      "v_accvgpr_read_b32 v68, a100\n\t"
      "v_accvgpr_read_b32 v69, a101\n\t"
      "v_accvgpr_read_b32 v70, a102\n\t"
      "v_accvgpr_read_b32 v71, a103\n\t"
      "v_mul_f32 v64, v64, v104\n\t"
      "v_mul_f32 v65, v65, v104\n\t"
      "v_mul_f32 v66, v66, v104\n\t"
      "v_mul_f32 v67, v67, v104\n\t"
      "v_mul_f32 v68, v68, v104\n\t"
      "v_mul_f32 v69, v69, v104\n\t"
      "v_mul_f32 v70, v70, v104\n\t"
      "v_mul_f32 v71, v71, v104\n\t"
      "v_cvt_pk_bf16_f32 v96, v64, v65\n\t"
      "v_cvt_pk_bf16_f32 v97, v66, v67\n\t"
      "v_cvt_pk_bf16_f32 v98, v68, v69\n\t"
      "v_cvt_pk_bf16_f32 v99, v70, v71\n\t"
      "s_add_u32 s42, %[rot], 6\n\t"
      "s_and_b32 s42, s42, 7\n\t"
      "s_lshl_b32 s42, s42, 17\n\t"
      "s_add_u32 s42, s42, %[so]\n\t"
      "buffer_store_dwordx4 v[96:99], %[ob], %[srd], s42 offen sc1\n\t"
      "v_accvgpr_read_b32 v104, a120\n\t"
      "v_mov_b32 v105, v104\n\t"
      "s_nop 1\n\t"
      "v_permlane32_swap_b32 v104, v105\n\t"
      "v_rcp_f32 v104, v104\n\t"
      "v_accvgpr_read_b32 v64, a112\n\t"
      "v_accvgpr_read_b32 v65, a113\n\t"
      "v_accvgpr_read_b32 v66, a114\n\t"
      "v_accvgpr_read_b32 v67, a115\n\t"
      "v_accvgpr_read_b32 v68, a116\n\t"
      "v_accvgpr_read_b32 v69, a117\n\t"
      "v_accvgpr_read_b32 v70, a118\n\t"
      "v_accvgpr_read_b32 v71, a119\n\t"
      "v_mul_f32 v64, v64, v104\n\t"
      "v_mul_f32 v65, v65, v104\n\t"
      "v_mul_f32 v66, v66, v104\n\t"
      "v_mul_f32 v67, v67, v104\n\t"
      "v_mul_f32 v68, v68, v104\n\t"
      "v_mul_f32 v69, v69, v104\n\t"
      "v_mul_f32 v70, v70, v104\n\t"
      "v_mul_f32 v71, v71, v104\n\t"
      "v_cvt_pk_bf16_f32 v96, v64, v65\n\t"
      "v_cvt_pk_bf16_f32 v97, v66, v67\n\t"
      "v_cvt_pk_bf16_f32 v98, v68, v69\n\t"
      "v_cvt_pk_bf16_f32 v99, v70, v71\n\t"
      "s_add_u32 s42, %[rot], 7\n\t"
      "s_and_b32 s42, s42, 7\n\t"
      "s_lshl_b32 s42, s42, 17\n\t"
      "s_add_u32 s42, s42, %[so]\n\t"
      "buffer_store_dwordx4 v[96:99], %[ob], %[srd], s42 offen sc1\n\t"
      "s_nop 1\n\t"
      : [kp] "+v"(kp), [vp] "+v"(vp)
      : [q0] "v"(Qf[0]), [q1] "v"(Qf[1]), [q2] "v"(Qf[2]), [q3] "v"(Qf[3]), [q4] "v"(Qf[4]), [q5] "v"(Qf[5]), [q6] "v"(Qf[6]), [q7] "v"(Qf[7]),
        [vs] "v"(vs), [ob] "v"(ob), [srd] "s"(srd), [so] "s"(so), [rot] "s"(rot)
      : "memory", "vcc", "scc",
        "v48", "v49", "v50", "v51", "v52", "v53", "v54", "v55", "v56", "v57", "v58", "v59", "v60", "v61", "v62", "v63", "v64", "v65", "v66", "v67", "v68",
        "v69", "v70", "v71", "v72", "v73", "v74", "v75", "v76", "v77", "v78", "v79", "v80", "v81", "v82", "v83", "v84", "v85", "v86", "v87", "v88", "v89",
        "v90", "v91", "v92", "v93", "v94", "v95", "v96", "v97", "v98", "v99", "v100", "v101", "v102", "v103", "v104", "v105", "v106", "v107", "v108", "v109",
        "v110", "v111", "v112", "v113", "v114", "v115", "v116", "v117", "v118", "v119", "v120", "v121", "v122", "v123", "v124", "v125", "v126", "v127",
        "v128", "v129", "v130", "v131", "v132", "v133", "v134", "v135", "v136", "v137", "v138", "v139", "v140", "v141", "v142", "v143", "v144", "v145",
        "v146", "v147", "v148", "v149", "v150", "v151", "v152", "v153", "v154", "v155", "v156", "v157", "v158", "v159", "v160", "v161", "v162", "v163",
        "v164", "v165", "v166", "v167", "v168", "v169", "v170", "v171", "v172", "v173", "v174", "v175", "v176", "v177", "v178", "v179", "v180", "v181",
        "v182", "v183", "v184", "v185", "v186", "v187", "v188", "v189", "v190", "v191", "v192", "v193", "v194", "v195", "v196", "v197", "v198", "v199",
        "v200", "v201", "v202", "v203", "v204", "v205", "v206", "v207", "v208", "v209", "v210", "v211", "v212", "v213", "v214", "v215", "v216", "v217",
        "v218", "v219", "v220", "v221", "v222", "v223", "v224", "v225", "v226", "v227", "v228", "v229", "v230", "v231", "v232", "v233", "v234", "v235",
        "v236", "v237", "v238", "v239", "v240", "v241", "v242", "v243", "v244", "v245", "v246", "v247", "v248", "v249", "v250", "v251", "v252", "v253",
        "v254", "v255", "a0", "a1", "a2", "a3", "a4", "a5", "a6", "a7", "a8", "a9", "a10", "a11", "a12", "a13", "a14", "a15", "a16", "a17", "a18", "a19",
        "a20", "a21", "a22", "a23", "a24", "a25", "a26", "a27", "a28", "a29", "a30", "a31", "a32", "a33", "a34", "a35", "a36", "a37", "a38", "a39", "a40",
        "a41", "a42", "a43", "a44", "a45", "a46", "a47", "a48", "a49", "a50", "a51", "a52", "a53", "a54", "a55", "a56", "a57", "a58", "a59", "a60", "a61",
        "a62", "a63", "a64", "a65", "a66", "a67", "a68", "a69", "a70", "a71", "a72", "a73", "a74", "a75", "a76", "a77", "a78", "a79", "a80", "a81", "a82",
        "a83", "a84", "a85", "a86", "a87", "a88", "a89", "a90", "a91", "a92", "a93", "a94", "a95", "a96", "a97", "a98", "a99", "a100", "a101", "a102", "a103",
        "a104", "a105", "a106", "a107", "a108", "a109", "a110", "a111", "a112", "a113", "a114", "a115", "a116", "a117", "a118", "a119", "a120", "a121",
        "a122", "a123", "a124", "a125", "a126", "a127", "s40", "s41", "s42", "s43", "s44", "s45");
  }
  b_out(p, ws, lds, hb, half, w);
}

__global__ void __launch_bounds__(256, 1) fused_kernel(Params p) {
  extern __shared__ __attribute__((aligned(16))) char smem[];
  lptr lds = (lptr)smem;
  const rsrc_t ws = __builtin_amdgcn_make_buffer_rsrc(p.ws, 0, WS_BYTES, 0x00020000);
  int x = (int)(__builtin_amdgcn_s_getreg(20 | (3 << 11)) & 7u), y = (int)(blockIdx.x >> 3);
  Tag tag;
  {
    typedef __attribute__((address_space(1))) unsigned long long gu64;
    gu64* clm = (gu64*)(p.ws + F_CLM);
    int role = x * 32 + y;
    const int guess = role;
    auto warm = [&]() {
      if (x < 4) warm_rows(p.wq, lds, x, y - 16);
      warm_rows(p.wk, lds, x & 3, y - 16); warm_rows(p.wv, lds, x & 3, y - 16);
      if (x < 4) { VMWAIT(32); warm_linear(p.wo, lds, x, y - 16); }
    };
    const int wv = rfl(threadIdx.x >> 6);
    if (y >= 16 && role != 16 && wv > 0) warm();
    if (threadIdx.x >= 64 && threadIdx.x < 128) {
      const int l = threadIdx.x - 64;
      const char* base = (const char*)(l < 16 ? p.wq : (l < 32 ? p.wk : (l < 48 ? p.wv : p.wo)));
      unsigned t = *(const unsigned*)(base + (size_t)(l & 15) * 262144 + (blockIdx.x & 31) * 256);
      t ^= *(const unsigned*)((const char*)p.ax + (size_t)(l & 31) * 65536);
      asm volatile("" :: "v"(t));
    } else if (threadIdx.x >= 128 && y == 31) {
      const int l = threadIdx.x - 128;
      unsigned t = 0;
#pragma unroll
      for (int i = 0; i < 9; ++i) t ^= gld4(ws, (unsigned)((i * 128 + l) * 65536 + 65408));
      t ^= *(const unsigned*)((const char*)p.out + (size_t)(l & 63) * 65536) ^ *(const unsigned*)((const char*)p.bx + (size_t)(l & 31) * 65536);
      t ^= *(const unsigned*)((const char*)p.bwqkv + (size_t)(l % 24) * 65536) ^ *(const unsigned*)((const char*)p.bwo + (size_t)(l & 7) * 65536);
      asm volatile("" :: "v"(t));
    }
    if (wv == 0) {
      const u32x2 e = gld8(ws, F_EPO);
      tag.lo = (unsigned)rfl((int)((e.x & ~0xFFu) + 0x100u)); tag.hi = (unsigned)rfl((int)e.y);
      const unsigned long long t = ((unsigned long long)tag.hi << 32) | tag.lo;
      if (threadIdx.x == 0 && __hip_atomic_exchange(clm + role * 8, t, __ATOMIC_RELAXED, __HIP_MEMORY_SCOPE_AGENT) == t) {
        for (role = 0; __hip_atomic_exchange(clm + role * 8, t, __ATOMIC_RELAXED, __HIP_MEMORY_SCOPE_AGENT) == t && role < 255; ++role) {}
      }
      role = rfl(role);
      if (role < 128 && (role & 16)) init_exchange(p, ws, (role & 15) * 4 + (role >> 5), tag);
      if (threadIdx.x == 0) *(LAS u32x4*)lds = (u32x4){(unsigned)role, tag.lo, tag.hi, 0u};
      if (y >= 16) warm();
    }
    LDS_BARRIER();
    const u32x4 r = *(LAS u32x4*)lds;
    role = rfl((int)r.x); tag.lo = (unsigned)rfl((int)r.y); tag.hi = (unsigned)rfl((int)r.z);
    if (role != guess) VMWAIT(0);
    LDS_BARRIER();
    x = role >> 5; y = role & 31;
  }
  const int xq = x & 3, yy = y & 15;
  if (y < 16) {
    if (x < 4) head_node(p, ws, lds, 8 * xq + (yy >> 1), yy & 1, tag);
    else {
      if (y < 8) { kv_worker(p, ws, lds, 8 * xq + y, tag); LDS_BARRIER(); }
      qa_worker(p, ws, lds, 8 * xq + (y & 7), y < 8 ? QA_SPLIT + 1 : 0, tag);
    }
  } else {
    if (x >= 4) { b_worker(p, ws, lds, (xq * 16 + yy) >> 1, yy & 1, tag); VM_LDS_BARRIER(0); }
    out_worker(p, ws, lds, yy, xq, x < 4, tag);
  }
}

extern "C" void kernel_launch(void* const* d_in, const int* in_sizes, int n_in, void* d_out, int out_size, void* d_ws, size_t ws_size, hipStream_t stream) {
  (void)in_sizes; (void)n_in; (void)out_size; (void)ws_size;
  Params p;
  p.ax = (const __bf16*)d_in[0]; p.mem0 = (const __bf16*)d_in[1]; p.wq = (const __bf16*)d_in[2]; p.wk = (const __bf16*)d_in[3];
  p.wv = (const __bf16*)d_in[4]; p.wo = (const __bf16*)d_in[5]; p.bx = (const __bf16*)d_in[7]; p.bwqkv = (const __bf16*)d_in[8];
  p.bwo = (const __bf16*)d_in[9]; p.out = (__bf16*)d_out;
  p.ws = (char*)(((size_t)d_ws + (2 * MB - 1)) & ~(size_t)(2 * MB - 1));
  (void)hipFuncSetAttribute((const void*)fused_kernel, hipFuncAttributeMaxDynamicSharedMemorySize, LDS_BYTES);
  fused_kernel<<<256, 256, LDS_BYTES, stream>>>(p);
}
```
